```python
import torch

def _softmax(x, axis=-1):
    return torch.softmax(x, dim=axis)
def _gelu(x):
    return torch.nn.functional.gelu(x, approximate="tanh")
def _silu(x):
    return torch.nn.functional.silu(x)
def _sigmoid(x):
    return torch.sigmoid(x)
def _layernorm(x, axis=-1, eps=1e-5):
    m = x.mean(dim=axis, keepdim=True)
    v = x.var(dim=axis, keepdim=True, unbiased=False)
    return (x - m) / torch.sqrt(v + eps)
def _scatter_add(tgt, idx, src, axis=0):
    return tgt.clone().index_add_(axis, idx, src)
def _segment_sum(src, seg, n):
    out = torch.zeros((n,) + src.shape[1:], dtype=src.dtype)
    return out.index_add_(0, seg, src)
def _pad(x, pw):
    flat = tuple(v for pair in reversed(pw) for v in pair)
    return torch.nn.functional.pad(x, flat)

kernel_name = 'recurrent_scan__linear_kernel_hedgehog__glu_gated_geglu__prompt'

def setup_inputs():

    _w = (lambda a: (lambda t: t.to(torch.bfloat16) if t.is_floating_point() else t)(torch.as_tensor(a)))
    def _A_setup():
        B, S, D, H = 1, 2048, 512, 32
        Dh = D // H
        return {
            "x": _w((lambda *s: torch.randn(*s, dtype=torch.bfloat16) * 0.05)(B, S, D)),
            "wqkv": _w((lambda *s: torch.randn(*s, dtype=torch.bfloat16) * 0.8)(D, 3 * D)),
            "w_phi": _w((lambda *s: torch.randn(*s, dtype=torch.bfloat16) * 0.8)(H, Dh, Dh) * (Dh ** -0.5)),
            "wo": _w((lambda *s: torch.randn(*s, dtype=torch.bfloat16) * 0.8)(D, D)),
        }
    _A_in = _A_setup()

    def _B_setup():
        B, S, Dm, Df = 1, 2048, 512, 1792
        return {
            "x": _w((lambda *s: torch.randn(*s, dtype=torch.bfloat16) * 0.05)(B, S, Dm)),
            "w_gate": _w((lambda *s: torch.randn(*s, dtype=torch.bfloat16) * 0.2)(Dm, Df) * (Dm ** -0.5)),
            "w_up": _w((lambda *s: torch.randn(*s, dtype=torch.bfloat16) * 0.2)(Dm, Df) * (Dm ** -0.5)),
            "w_down": _w((lambda *s: torch.randn(*s, dtype=torch.bfloat16) * 0.2)(Df, Dm) * (Df ** -0.5)),
        }
    _B_in = _B_setup()

    return {
        "A_x": _A_in["x"],
        "A_wqkv": _A_in["wqkv"],
        "A_w_phi": _A_in["w_phi"],
        "A_wo": _A_in["wo"],
        "B_x": _B_in["x"],
        "B_w_gate": _B_in["w_gate"],
        "B_w_up": _B_in["w_up"],
        "B_w_down": _B_in["w_down"],
    }

def reference(A_x, A_wqkv, A_w_phi, A_wo, B_x, B_w_gate, B_w_up, B_w_down):
    def _A_ref(x, wqkv, w_phi, wo):
        B, S, D = x.shape
        H = 32; Dh = D // H
        qkv = (lambda x, *d: x.permute(*d))((x @ wqkv).reshape(B, S, 3, H, Dh), 2, 0, 3, 1, 4)
        q, k, v = qkv[0], qkv[1], qkv[2]
        pq = torch.einsum("bhsd,hdf->bhsf", q, w_phi)
        pk = torch.einsum("bhsd,hdf->bhsf", k, w_phi)
        phi_q = torch.concatenate([_softmax(pq, axis=-1), _softmax(-pq, axis=-1)], axis=-1)
        phi_k = torch.concatenate([_softmax(pk, axis=-1), _softmax(-pk, axis=-1)], axis=-1)
        kv = torch.einsum("bhsf,bhsd->bhfd", phi_k, v)
        z  = torch.einsum("bhsf->bhf", phi_k)
        num = torch.einsum("bhqf,bhfd->bhqd", phi_q, kv)
        den = torch.einsum("bhqf,bhf->bhq", phi_q, z)[..., None] + 1e-6
        o = num / den
        return (lambda x, *d: x.permute(*d))(o, 0, 2, 1, 3).reshape(B, S, D) @ wo

    def _B_ref(x, w_gate, w_up, w_down):
        g = torch.einsum("bsd,df->bsf", x, w_gate)
        u = torch.einsum("bsd,df->bsf", x, w_up)
        h = _gelu(g) * u
        return torch.einsum("bsf,fd->bsd", h, w_down)

    x = A_x
    xn = _layernorm(x, axis=-1)
    s = _A_ref(xn, A_wqkv, A_w_phi, A_wo)
    g = _silu(_B_ref(xn, B_w_gate, B_w_up, B_w_down))
    return x + s * g
```

```cpp
#include <hip/hip_runtime.h>
#include <hip/hip_bf16.h>
typedef unsigned short u16;
static constexpr int S=2048,D=512,H=32,DH=16,DF=1792;
__device__ __forceinline__ float b2f(u16 v){return __uint_as_float(((unsigned)v)<<16);}
__device__ __forceinline__ u16 f2b(float f){unsigned u=__float_as_uint(f);u+=0x7fff+((u>>16)&1);return (u16)(u>>16);}
__device__ __forceinline__ float rb(float f){return b2f(f2b(f));}

__global__ void k_ln(const u16* x,u16* xn){
  int t=blockIdx.x; int l=threadIdx.x; float v[8]; float s=0;
  for(int j=0;j<8;j++){v[j]=b2f(x[t*D+l*8+j]); s+=v[j];}
  for(int o=32;o;o>>=1) s+=__shfl_xor(s,o);
  float m=s/D; float q=0; for(int j=0;j<8;j++){float d=v[j]-m;q+=d*d;}
  for(int o=32;o;o>>=1) q+=__shfl_xor(q,o);
  float r=rsqrtf(q/D+1e-5f);
  for(int j=0;j<8;j++) xn[t*D+l*8+j]=f2b((v[j]-m)*r);
}
__global__ void k_gemm(const u16* A,const u16* B,float* C,int N,int K){
  int n=blockIdx.x*256+threadIdx.x; int t0=blockIdx.y*8;
  float acc[8]={0,0,0,0,0,0,0,0};
  for(int k=0;k<K;k++){float b=b2f(B[(size_t)k*N+n]);
#pragma unroll
    for(int i=0;i<8;i++) acc[i]+=b2f(A[(size_t)(t0+i)*K+k])*b;}
#pragma unroll
  for(int i=0;i<8;i++) C[(size_t)(t0+i)*N+n]=acc[i];
}
__global__ void k_phi(const float* qkv,const u16* wphi,u16* phi){
  int idx=blockIdx.x*256+threadIdx.x; int f=idx&15; int h=(idx>>4)&31; int w=(idx>>9)&1; int t=idx>>10;
  float p=0;
  for(int d=0;d<16;d++) p+=rb(qkv[t*1536+w*512+h*16+d])*b2f(wphi[h*256+d*16+f]);
  float mx=p,mn=p;
  for(int o=8;o;o>>=1){mx=fmaxf(mx,__shfl_xor(mx,o));mn=fminf(mn,__shfl_xor(mn,o));}
  float e1=__expf(p-mx),e2=__expf(mn-p); float s1=e1,s2=e2;
  for(int o=8;o;o>>=1){s1+=__shfl_xor(s1,o);s2+=__shfl_xor(s2,o);}
  phi[((size_t)(w*S+t)*H+h)*32+f]=f2b(e1/s1);
  phi[((size_t)(w*S+t)*H+h)*32+16+f]=f2b(e2/s2);
}
__global__ void k_kv(const float* qkv,const u16* phi,float* kv){
  int h=blockIdx.x; int i=threadIdx.x; if(i>=544) return; int f=i/17,d=i%17; float a=0;
  for(int t=0;t<S;t++){float p=b2f(phi[((size_t)(S+t)*H+h)*32+f]); float v=d<16?rb(qkv[t*1536+1024+h*16+d]):1.f; a+=p*v;}
  kv[h*544+i]=a;
}
__global__ void k_o(const u16* phi,const float* kv,u16* o){
  int idx=blockIdx.x*256+threadIdx.x; int d=idx&15,h=(idx>>4)&31,t=idx>>9; float num=0,den=0;
  for(int f=0;f<32;f++){float p=b2f(phi[((size_t)t*H+h)*32+f]); num+=p*rb(kv[h*544+f*17+d]); den+=p*rb(kv[h*544+f*17+16]);}
  o[idx]=f2b(num/(den+1e-6f));
}
__global__ void k_h(const float* g,const float* u,u16* hh){
  int idx=blockIdx.x*256+threadIdx.x; float a=g[idx]; float t=tanhf(0.7978845608f*(a+0.044715f*a*a*a));
  hh[idx]=f2b(0.5f*a*(1.f+t)*u[idx]);
}
__global__ void k_fin(const u16* x,const float* s,const float* b,u16* out){
  int idx=blockIdx.x*256+threadIdx.x; float bb=b[idx]; float g=bb/(1.f+__expf(-bb));
  out[idx]=f2b(b2f(x[idx])+s[idx]*g);
}
extern "C" void kernel_launch(void* const* d_in,const int* in_sizes,int n_in,void* d_out,int out_size,void* d_ws,size_t ws_size,hipStream_t stream){
  const u16* x=(const u16*)d_in[0]; const u16* wqkv=(const u16*)d_in[1]; const u16* wphi=(const u16*)d_in[2]; const u16* wo=(const u16*)d_in[3];
  const u16* wg=(const u16*)d_in[5]; const u16* wu=(const u16*)d_in[6]; const u16* wd=(const u16*)d_in[7];
  char* w=(char*)d_ws; size_t M=1<<20;
  u16* xn=(u16*)w; float* qkv=(float*)(w+2*M); u16* phi=(u16*)(w+16*M); float* kv=(float*)(w+26*M); u16* o=(u16*)(w+27*M);
  float* g=(float*)(w+32*M); float* u=(float*)(w+48*M); u16* hh=(u16*)(w+64*M); float* s=(float*)(w+72*M); float* b=(float*)(w+80*M);
  k_ln<<<S,64,0,stream>>>(x,xn);
  k_gemm<<<dim3(1536/256,S/8),256,0,stream>>>(xn,wqkv,qkv,1536,512);
  k_phi<<<S*1024/256,256,0,stream>>>(qkv,wphi,phi);
  k_kv<<<H,576,0,stream>>>(qkv,phi,kv);
  k_o<<<S*512/256,256,0,stream>>>(phi,kv,o);
  k_gemm<<<dim3(DF/256,S/8),256,0,stream>>>(xn,wg,g,DF,512);
  k_gemm<<<dim3(DF/256,S/8),256,0,stream>>>(xn,wu,u,DF,512);
  k_h<<<S*DF/256,256,0,stream>>>(g,u,hh);
  k_gemm<<<dim3(2,S/8),256,0,stream>>>(o,wo,s,512,512);
  k_gemm<<<dim3(2,S/8),256,0,stream>>>(hh,wd,b,512,DF);
  k_fin<<<S*512/256,256,0,stream>>>(x,s,b,(u16*)d_out);
}
```

```cpp
#include <hip/hip_runtime.h>

struct Buf { const void* p; unsigned bytes; unsigned fmt; };
struct Args { Buf x, wqkv, wphi, wo, wg, wu, wd, out, ws; };

__global__ __launch_bounds__(512, 2) void fused_block(Args a) {
  __shared__ __attribute__((aligned(1024))) char lds[163840];
  unsigned tid = threadIdx.x;
  asm volatile(
    ".Lfz_KSTART:\n"
    "s_getpc_b64 s[54:55]\n"
    "s_load_dwordx16 s[12:27], %1, 0x0\n"
    "s_load_dwordx16 s[28:43], %1, 0x40\n"
    "s_load_dwordx4 s[44:47], %1, 0x80\n"
    "v_readfirstlane_b32 s48, v0\n"
    "v_and_b32 v0, 63, v0\n"
    "s_lshr_b32 s48, s48, 6\n"
    "s_lshr_b32 s98, %2, 3\n"
    "s_and_b32 s87, s98, 15\n"
    "s_lshl_b32 s87, s87, 3\n"
    "s_add_u32 s87, s87, s48\n"
    "s_lshl_b32 s87, s87, 7\n"
    "v_and_b32 v63, 3, v0\n"
    "v_lshlrev_b32 v63, 14, v63\n"
    "v_add_u32 v63, s87, v63\n"
    "v_min_u32 v63, .Lfz_KEND-.Lfz_KSTART-64, v63\n"
    "global_load_dword v63, v63, s[54:55]\n"
    "s_and_b32 s88, %2, 7\n"
    "s_lshr_b32 s49, s88, 2\n"
    "s_lshl_b32 s49, s49, 3\n"
    "s_and_b32 s95, s98, 7\n"
    "s_add_u32 s49, s49, s95\n"
    "s_lshr_b32 s94, s98, 3\n"
    "s_and_b32 s87, s88, 3\n"
    "s_lshl_b32 s87, s87, 2\n"
    "s_add_u32 s94, s94, s87\n"
    "s_lshl_b32 s87, s49, 4\n"
    "s_add_u32 s87, s87, s94\n"
    "s_lshl_b32 s87, s87, 3\n"
    "s_add_u32 s87, s87, s48\n"
    "s_lshl_b32 s8, s87, 10\n"
    "v_lshlrev_b32 v33, 4, v0\n"
    "s_mov_b32 s87, 0xd68000\n"
    "s_waitcnt lgkmcnt(0)\n"
    "buffer_load_dwordx4 v[64:67], v33, s[12:15], s8 offen\n"
    "buffer_load_dword v13, off, s[44:47], s87 sc1\n"
    "s_and_b32 s51, s94, 1\n"
    "s_lshr_b32 s50, s94, 1\n"
    "s_and_b32 s85, s48, 3\n"
    "s_lshr_b32 s84, s48, 2\n"
    "s_cmp_eq_u32 s84, 1\n"
    "s_cselect_b64 vcc, -1, 0\n"
    "s_lshl_b32 s52, s48, 10\n"
    "s_add_u32 s56, s95, 0\n"
    "s_and_b32 s56, s56, 7\n"
    "s_add_u32 s57, s95, 1\n"
    "s_and_b32 s57, s57, 7\n"
    "s_add_u32 s58, s95, 2\n"
    "s_and_b32 s58, s58, 7\n"
    "s_add_u32 s59, s95, 3\n"
    "s_and_b32 s59, s59, 7\n"
    "s_add_u32 s60, s95, 4\n"
    "s_and_b32 s60, s60, 7\n"
    "s_add_u32 s61, s95, 5\n"
    "s_and_b32 s61, s61, 7\n"
    "s_add_u32 s62, s95, 6\n"
    "s_and_b32 s62, s62, 7\n"
    "s_add_u32 s63, s95, 7\n"
    "s_and_b32 s63, s63, 7\n"
    "v_lshrrev_b32 v48, 3, v0\n"
    "v_and_b32 v49, 7, v0\n"
    "v_lshrrev_b32 v40, 4, v0\n"
    "v_and_b32 v44, 1, v40\n"
    "s_lshl_b32 s87, s48, 3\n"
    "v_add_u32 v48, s87, v48\n"
    "s_and_b32 s9, s48, 1\n"
    "s_lshl_b32 s9, s9, 2\n"
    "v_lshl_or_b32 v45, v44, 1, s9\n"
    "v_xor_b32 v45, v45, v49\n"
    "s_lshl_b32 s87, s50, 7\n"
    "v_lshl_add_u32 v45, v45, 4, s87\n"
    "s_movk_i32 s87, 3584\n"
    "v_mad_u32_u24 v6, v48, s87, v45\n"
    "s_movk_i32 s87, 3072\n"
    "v_mad_u32_u24 v7, v48, s87, v45\n"
    "s_lshl_b32 s87, s49, 4\n"
    "s_add_u32 s11, s87, s48\n"
    "s_lshl_b32 s11, s11, 7\n"
    "s_add_u32 s11, s11, 0xb60000\n"
    "s_add_u32 s87, s87, s94\n"
    "s_lshl_b32 s87, s87, 7\n"
    "s_add_u32 s10, s87, 0xb60000\n"
    "s_add_u32 s80, s87, 0x900000\n"
    "s_cmp_eq_u32 s48, 0\n"
    "s_cselect_b32 s10, s10, 0x40000000\n"
    "s_cselect_b32 s80, s80, 0x40000000\n"
    "s_add_u32 s8, s8, 0xb68000\n"
    "s_mov_b32 s99, 0x3f803f80\n"
    "v_lshlrev_b32 v53, 10, v0\n"
    "v_lshlrev_b32 v54, 2, v0\n"
    "s_cmp_eq_u32 s51, 1\n"
    "s_cbranch_scc1 .Lfz_PATH_Y\n"
    "s_add_u32 m0, s52, 0x0\n"
    "s_mul_i32 s54, s56, 0x38000\n"
    "buffer_load_dwordx4 v6, s[28:31], s54 offen lds\n"
    "s_add_u32 m0, s52, 0x2000\n"
    "s_mul_i32 s55, s56, 0x38000\n"
    "buffer_load_dwordx4 v6, s[32:35], s55 offen lds\n"
    "s_add_u32 m0, s52, 0x24000\n"
    "s_lshl_b32 s87, s50, 1\n"
    "s_add_u32 s87, s87, s84\n"
    "s_lshl_b32 s87, s87, 10\n"
    "buffer_load_dwordx4 v33, s[20:23], s87 offen lds\n"
    "v_mov_b32 v14, 0\n"
    "s_mov_b64 exec, 3\n"
    "buffer_atomic_or v14, v53, s[44:47], s11 offen sc0\n"
    "s_mov_b64 exec, -1\n"
    "s_lshl_b32 s87, s50, 3\n"
    "s_and_b32 s94, s49, 7\n"
    "s_add_u32 s87, s87, s94\n"
    "s_lshl_b32 s87, s87, 7\n"
    "s_and_b32 s94, s49, 8\n"
    "s_lshl_b32 s94, s94, 10\n"
    "s_add_u32 s87, s87, s94\n"
    "s_add_u32 s87, s87, 0xb5c000\n"
    "s_cmp_eq_u32 s48, 1\n"
    "s_cselect_b32 s80, s87, s80\n"
    "s_lshl_b32 s83, s88, 2\n"
    "s_and_b32 s87, s98, 3\n"
    "s_add_u32 s83, s83, s87\n"
    "s_lshr_b32 s82, s98, 2\n"
    "s_mov_b32 s89, 0x8000\n"
    "s_mov_b32 s4, 0xff00ff00\n"
    "s_mov_b32 s5, 0xff00ff00\n"
    "s_lshl_b32 s86, s49, 17\n"
    "s_add_u32 s86, s86, 0xb68000\n"
    "v_mov_b32 v12, 1\n"
    "v_bfe_u32 v41, v0, 2, 2\n"
    "v_and_b32 v42, 3, v0\n"
    "v_lshlrev_b32 v42, 3, v42\n"
    "v_lshl_add_u32 v43, v40, 3, v41\n"
    "v_bfe_u32 v45, v0, 3, 2\n"
    "s_lshl_b32 s87, s84, 1\n"
    "v_xor_b32 v45, s87, v45\n"
    "v_lshl_or_b32 v45, v45, 5, v42\n"
    "v_lshl_or_b32 v1, v43, 7, v45\n"
    "v_xor_b32 v2, 32, v1\n"
    "s_setprio 1\n"
    "v_mov_b32 v76, 0\n"
    "v_mov_b32 v78, 0\n"
    "s_waitcnt vmcnt(5)\n"
    "v_dot2c_f32_bf16 v76, s99, v64\n"
    "v_dot2c_f32_bf16 v78, v64, v64\n"
    "v_dot2c_f32_bf16 v76, s99, v65\n"
    "v_dot2c_f32_bf16 v78, v65, v65\n"
    "v_dot2c_f32_bf16 v76, s99, v66\n"
    "v_dot2c_f32_bf16 v78, v66, v66\n"
    "v_dot2c_f32_bf16 v76, s99, v67\n"
    "v_dot2c_f32_bf16 v78, v67, v67\n"
    "v_lshlrev_b32 v68, 16, v64\n"
    "v_and_b32 v69, 0xffff0000, v64\n"
    "v_lshlrev_b32 v70, 16, v65\n"
    "v_permlane32_swap_b32 v76, v78\n"
    "v_add_f32 v76, v76, v78\n"
    "v_and_b32 v71, 0xffff0000, v65\n"
    "v_lshlrev_b32 v72, 16, v66\n"
    "v_add_f32_dpp v76, v76, v76 row_ror:8 row_mask:0xf bank_mask:0xf\n"
    "v_and_b32 v73, 0xffff0000, v66\n"
    "v_lshlrev_b32 v74, 16, v67\n"
    "v_add_f32_dpp v76, v76, v76 row_ror:4 row_mask:0xf bank_mask:0xf\n"
    "v_and_b32 v75, 0xffff0000, v67\n"
    "s_waitcnt vmcnt(4)\n"
    "v_readfirstlane_b32 s53, v13\n"
    "v_add_f32_dpp v76, v76, v76 row_ror:2 row_mask:0xf bank_mask:0xf\n"
    "s_mul_i32 s53, s53, 1664525\n"
    "s_add_u32 s53, s53, 1013904223\n"
    "v_add_f32_dpp v76, v76, v76 row_ror:1 row_mask:0xf bank_mask:0xf\n"
    "v_mov_b32 v78, v76\n"
    "v_mov_b32 v13, s53\n"
    "s_nop 0\n"
    "v_permlane16_swap_b32 v76, v78\n"
    "v_add_f32 v76, v76, v78\n"
    "v_mul_f32 v76, 0x3b000000, v76\n"
    "v_mov_b32 v78, v76\n"
    "s_nop 1\n"
    "v_permlane32_swap_b32 v76, v78\n"
    "v_fma_f32 v78, -v76, v76, v78\n"
    "v_max_f32 v78, 0, v78\n"
    "v_cvt_pk_bf16_f32 v77, v76, v78\n"
    "v_and_b32 v78, 0xffff0000, v77\n"
    "v_lshlrev_b32 v76, 16, v77\n"
    "v_add_f32 v78, 0x3727c5ac, v78\n"
    "v_pk_add_f32 v[68:69], v[68:69], v[76:77] op_sel_hi:[1,0] neg_lo:[0,1] neg_hi:[0,1]\n"
    "v_cvt_pk_bf16_f32 v78, v78, v78\n"
    "v_pk_add_f32 v[70:71], v[70:71], v[76:77] op_sel_hi:[1,0] neg_lo:[0,1] neg_hi:[0,1]\n"
    "v_and_b32 v78, 0xffff0000, v78\n"
    "v_pk_add_f32 v[72:73], v[72:73], v[76:77] op_sel_hi:[1,0] neg_lo:[0,1] neg_hi:[0,1]\n"
    "v_sqrt_f32 v78, v78\n"
    "v_pk_add_f32 v[74:75], v[74:75], v[76:77] op_sel_hi:[1,0] neg_lo:[0,1] neg_hi:[0,1]\n"
    "v_cvt_pk_bf16_f32 v78, v78, v78\n"
    "v_cvt_pk_bf16_f32 v64, v68, v69\n"
    "v_and_b32 v78, 0xffff0000, v78\n"
    "v_cvt_pk_bf16_f32 v65, v70, v71\n"
    "v_rcp_f32 v78, v78\n"
    "v_cvt_pk_bf16_f32 v66, v72, v73\n"
    "v_cvt_pk_bf16_f32 v67, v74, v75\n"
    "v_lshlrev_b32 v68, 16, v64\n"
    "v_and_b32 v69, 0xffff0000, v64\n"
    "v_lshlrev_b32 v70, 16, v65\n"
    "v_and_b32 v71, 0xffff0000, v65\n"
    "v_lshlrev_b32 v72, 16, v66\n"
    "v_and_b32 v73, 0xffff0000, v66\n"
    "v_lshlrev_b32 v74, 16, v67\n"
    "v_and_b32 v75, 0xffff0000, v67\n"
    "v_pk_mul_f32 v[68:69], v[68:69], v[78:79] op_sel_hi:[1,0]\n"
    "v_pk_mul_f32 v[70:71], v[70:71], v[78:79] op_sel_hi:[1,0]\n"
    "v_pk_mul_f32 v[72:73], v[72:73], v[78:79] op_sel_hi:[1,0]\n"
    "v_pk_mul_f32 v[74:75], v[74:75], v[78:79] op_sel_hi:[1,0]\n"
    "v_cvt_pk_bf16_f32 v64, v68, v69\n"
    "v_cvt_pk_bf16_f32 v65, v70, v71\n"
    "v_cvt_pk_bf16_f32 v66, v72, v73\n"
    "v_cvt_pk_bf16_f32 v67, v74, v75\n"
    "buffer_store_dwordx4 v[64:67], v33, s[44:47], s8 offen sc1\n"
    "s_mov_b32 exec_hi, 0\n"
    "buffer_store_dword v13, v54, s[44:47], s80 offen sc1\n"
    "s_mov_b64 exec, -1\n"
    "s_setprio 0\n"
    "s_add_u32 m0, s52, 0x4000\n"
    "s_mul_i32 s54, s57, 0x38000\n"
    "buffer_load_dwordx4 v6, s[28:31], s54 offen lds\n"
    "s_add_u32 m0, s52, 0x6000\n"
    "s_mul_i32 s55, s57, 0x38000\n"
    "buffer_load_dwordx4 v6, s[32:35], s55 offen lds\n"
    "v_add_u32 v45, s9, v40\n"
    "v_xor_b32 v45, v45, v49\n"
    "v_lshlrev_b32 v45, 4, v45\n"
    "v_lshl_or_b32 v32, v48, 10, v45\n"
    "v_bfe_u32 v45, v0, 1, 3\n"
    "v_xor_b32 v45, v45, v40\n"
    "v_lshlrev_b32 v45, 4, v45\n"
    "v_and_b32 v46, 15, v0\n"
    "v_lshl_or_b32 v45, v46, 7, v45\n"
    "s_lshl_b32 s87, s85, 12\n"
    "v_add_u32 v4, s87, v45\n"
    "v_xor_b32 v5, 64, v4\n"
    "v_lshlrev_b32 v45, 1, v40\n"
    "v_xor_b32 v45, v45, v49\n"
    "v_lshlrev_b32 v45, 4, v45\n"
    "v_lshl_or_b32 v45, v48, 10, v45\n"
    "s_lshl_b32 s87, s82, 7\n"
    "v_add_u32 v9, s87, v45\n"
    "s_lshl_b32 s95, s83, 1\n"
    "s_add_u32 s95, s95, s84\n"
    "s_mul_i32 s87, s95, 0x24000\n"
    "s_lshl_b32 s94, s85, 11\n"
    "s_add_u32 s87, s87, s94\n"
    "v_add_u32 v11, s87, v33\n"
    "s_lshl_b32 s87, s95, 2\n"
    "s_add_u32 s87, s87, s85\n"
    "s_lshl_b32 s87, s87, 7\n"
    "s_add_u32 s6, s87, 0x900000\n"
    "s_lshl_b32 s95, s49, 2\n"
    "s_add_u32 s95, s95, s85\n"
    "s_mul_i32 s87, s95, 0x24000\n"
    "s_lshl_b32 s94, s84, 11\n"
    "s_add_u32 s87, s87, s94\n"
    "v_add_u32 v10, s87, v33\n"
    "s_lshl_b32 s95, s95, 9\n"
    "s_add_u32 s95, s95, 0x900000\n"
    "s_waitcnt vmcnt(2)\n"
    "s_barrier\n"
    "s_mov_b32 exec_hi, 0\n"
    "buffer_store_dword v13, v54, s[44:47], s10 offen sc1\n"
    "s_mov_b64 exec, -1\n"
    "s_add_u32 m0, s52, 0x8000\n"
    "s_mul_i32 s54, s58, 0x38000\n"
    "buffer_load_dwordx4 v6, s[28:31], s54 offen lds\n"
    "s_add_u32 m0, s52, 0xa000\n"
    "s_mul_i32 s55, s58, 0x38000\n"
    "buffer_load_dwordx4 v6, s[32:35], s55 offen lds\n"
    "s_and_b32 s87, s50, 1\n"
    "s_lshl_b32 s87, s87, 1\n"
    "s_add_u32 s87, s87, s84\n"
    "s_lshl_b32 s87, s87, 7\n"
    "s_add_u32 s64, s87, s95\n"
    "s_lshl_b32 s67, s50, 12\n"
    "v_bfe_u32 v51, v0, 1, 1\n"
    "v_lshlrev_b32 v51, 9, v51\n"
    "v_lshrrev_b32 v45, 2, v0\n"
    "v_lshl_or_b32 v51, v45, 5, v51\n"
    "v_add_u32 v51, s52, v51\n"
    "v_bfe_i32 v52, v0, 2, 1\n"
    "v_and_b32 v52, 0x80008000, v52\n"
    "v_mov_b32 v14, 0\n"
    "s_mov_b64 exec, 3\n"
    "buffer_atomic_or v14, v53, s[44:47], s11 offen sc0\n"
    "s_mov_b64 exec, -1\n"
    "s_add_u32 m0, s52, 0xc000\n"
    "s_mul_i32 s54, s59, 0x38000\n"
    "buffer_load_dwordx4 v6, s[28:31], s54 offen lds\n"
    "s_add_u32 m0, s52, 0xe000\n"
    "s_mul_i32 s55, s59, 0x38000\n"
    "buffer_load_dwordx4 v6, s[32:35], s55 offen lds\n"
    "s_add_u32 m0, s52, 0x10000\n"
    "s_mul_i32 s54, s60, 0x38000\n"
    "buffer_load_dwordx4 v6, s[28:31], s54 offen lds\n"
    "s_add_u32 m0, s52, 0x12000\n"
    "s_mul_i32 s55, s60, 0x38000\n"
    "buffer_load_dwordx4 v6, s[32:35], s55 offen lds\n"
    "s_lshl_b32 s87, s50, 3\n"
    "s_lshr_b32 s94, s49, 1\n"
    "s_add_u32 s94, s87, s94\n"
    "s_lshl_b32 s94, s94, 7\n"
    "s_add_u32 s70, s94, 0xb5c000\n"
    "s_add_u32 s87, s87, s48\n"
    "s_mul_i32 s78, s87, 1280\n"
    "s_add_u32 s78, s78, 0xb48000\n"
    "s_lshl_b32 s87, s87, 7\n"
    "s_add_u32 s77, s87, 0xb5e000\n"
    "v_add_u32 v51, 0x24000, v51\n"
    "ds_read_b64_tr_b16 v[34:35], v51 offset:0\n"
    "ds_read_b64_tr_b16 v[36:37], v51 offset:8\n"
    "ds_read_b64_tr_b16 v[38:39], v51 offset:16\n"
    "ds_read_b64_tr_b16 v[40:41], v51 offset:24\n"
    "s_waitcnt lgkmcnt(3)\n"
    "v_xor_b32 v34, v52, v34\n"
    "v_xor_b32 v35, v52, v35\n"
    "s_waitcnt lgkmcnt(2)\n"
    "v_xor_b32 v36, v52, v36\n"
    "v_xor_b32 v37, v52, v37\n"
    "s_waitcnt lgkmcnt(1)\n"
    "v_xor_b32 v38, v52, v38\n"
    "v_xor_b32 v39, v52, v39\n"
    "s_waitcnt lgkmcnt(0)\n"
    "v_xor_b32 v40, v52, v40\n"
    "v_xor_b32 v41, v52, v41\n"
    "s_waitcnt vmcnt(4)\n"
    "v_cmp_eq_u32 s[90:91], s53, v14\n"
    "s_and_b32 s90, s90, 3\n"
    "s_cmp_eq_u32 s90, 3\n"
    "s_cbranch_scc0 .Lfz_SLOW_X_0\n"
    ".Lfz_RET_X_0:\n"
    "s_add_u32 m0, s52, 0x14000\n"
    "s_lshl_b32 s54, s56, 7\n"
    "s_add_u32 s54, s54, s86\n"
    "buffer_load_dwordx4 v32, s[44:47], s54 offen sc1 lds\n"
    "s_add_u32 m0, s52, 0x16000\n"
    "s_lshl_b32 s55, s56, 7\n"
    "s_add_u32 s55, s55, s86\n"
    "s_add_u32 s55, s55, 0x10000\n"
    "buffer_load_dwordx4 v32, s[44:47], s55 offen sc1 lds\n"
    "s_add_u32 m0, s52, 0x18000\n"
    "s_lshl_b32 s54, s57, 7\n"
    "s_add_u32 s54, s54, s86\n"
    "buffer_load_dwordx4 v32, s[44:47], s54 offen sc1 lds\n"
    "s_add_u32 m0, s52, 0x1a000\n"
    "s_lshl_b32 s55, s57, 7\n"
    "s_add_u32 s55, s55, s86\n"
    "s_add_u32 s55, s55, 0x10000\n"
    "buffer_load_dwordx4 v32, s[44:47], s55 offen sc1 lds\n"
    "s_add_u32 m0, s52, 0x1c000\n"
    "s_lshl_b32 s54, s58, 7\n"
    "s_add_u32 s54, s54, s86\n"
    "buffer_load_dwordx4 v32, s[44:47], s54 offen sc1 lds\n"
    "s_add_u32 m0, s52, 0x1e000\n"
    "s_lshl_b32 s55, s58, 7\n"
    "s_add_u32 s55, s55, s86\n"
    "s_add_u32 s55, s55, 0x10000\n"
    "buffer_load_dwordx4 v32, s[44:47], s55 offen sc1 lds\n"
    "s_waitcnt vmcnt(2)\n"
    "s_cbranch_vccz .Lfz_RET_bX1\n"
    "s_barrier\n"
    ".Lfz_RET_bX1:\n"
    "s_add_u32 m0, s52, 0x20000\n"
    "s_lshl_b32 s54, s59, 7\n"
    "s_add_u32 s54, s54, s86\n"
    "buffer_load_dwordx4 v32, s[44:47], s54 offen sc1 lds\n"
    "s_add_u32 m0, s52, 0x22000\n"
    "s_lshl_b32 s55, s59, 7\n"
    "s_add_u32 s55, s55, s86\n"
    "s_add_u32 s55, s55, 0x10000\n"
    "buffer_load_dwordx4 v32, s[44:47], s55 offen sc1 lds\n"
    "s_add_u32 m0, s52, 0x24000\n"
    "s_lshl_b32 s54, s60, 7\n"
    "s_add_u32 s54, s54, s86\n"
    "buffer_load_dwordx4 v32, s[44:47], s54 offen sc1 lds\n"
    "s_add_u32 m0, s52, 0x26000\n"
    "s_lshl_b32 s55, s60, 7\n"
    "s_add_u32 s55, s55, s86\n"
    "s_add_u32 s55, s55, 0x10000\n"
    "buffer_load_dwordx4 v32, s[44:47], s55 offen sc1 lds\n"
    "s_cbranch_vccnz .Lfz_RET_bX2\n"
    "s_barrier\n"
    ".Lfz_RET_bX2:\n"
    "v_add_u32 v4, 0x14000, v4\n"
    "ds_read_b128 v[128:131], v4 offset:0\n"
    "ds_read_b128 v[132:135], v4 offset:2048\n"
    "v_add_u32 v5, 0x14000, v5\n"
    "ds_read_b128 v[136:139], v5 offset:0\n"
    "ds_read_b128 v[140:143], v5 offset:2048\n"
    "ds_read_b64_tr_b16 v[16:17], v1 offset:0\n"
    "ds_read_b64_tr_b16 v[18:19], v1 offset:512\n"
    "ds_read_b64_tr_b16 v[20:21], v2 offset:0\n"
    "ds_read_b64_tr_b16 v[22:23], v2 offset:512\n"
    "ds_read_b64_tr_b16 v[24:25], v1 offset:8192\n"
    "ds_read_b64_tr_b16 v[26:27], v1 offset:8704\n"
    "s_waitcnt lgkmcnt(4)\n"
    "v_mfma_f32_16x16x32_bf16 v[64:67], v[16:19], v[128:131], 0\n"
    "ds_read_b64_tr_b16 v[28:29], v2 offset:8192\n"
    "v_mfma_f32_16x16x32_bf16 v[68:71], v[16:19], v[132:135], 0\n"
    "ds_read_b64_tr_b16 v[30:31], v2 offset:8704\n"
    "ds_read_b128 v[144:147], v4 offset:16384\n"
    "s_waitcnt lgkmcnt(5)\n"
    "v_mfma_f32_16x16x32_bf16 v[76:79], v[20:23], v[132:135], 0\n"
    "ds_read_b64_tr_b16 v[16:17], v1 offset:4096\n"
    "v_mfma_f32_16x16x32_bf16 v[72:75], v[20:23], v[128:131], 0\n"
    "ds_read_b64_tr_b16 v[18:19], v1 offset:4608\n"
    "ds_read_b128 v[148:151], v4 offset:18432\n"
    "s_waitcnt lgkmcnt(6)\n"
    "v_mfma_f32_16x16x32_bf16 v[80:83], v[24:27], v[128:131], 0\n"
    "ds_read_b64_tr_b16 v[20:21], v2 offset:4096\n"
    "v_mfma_f32_16x16x32_bf16 v[84:87], v[24:27], v[132:135], 0\n"
    "ds_read_b64_tr_b16 v[22:23], v2 offset:4608\n"
    "ds_read_b128 v[152:155], v5 offset:16384\n"
    "s_waitcnt lgkmcnt(7)\n"
    "v_mfma_f32_16x16x32_bf16 v[92:95], v[28:31], v[132:135], 0\n"
    "ds_read_b64_tr_b16 v[24:25], v1 offset:12288\n"
    "v_mfma_f32_16x16x32_bf16 v[88:91], v[28:31], v[128:131], 0\n"
    "ds_read_b64_tr_b16 v[26:27], v1 offset:12800\n"
    "ds_read_b128 v[156:159], v5 offset:18432\n"
    "s_waitcnt lgkmcnt(7)\n"
    "v_mfma_f32_16x16x32_bf16 v[64:67], v[16:19], v[136:139], v[64:67]\n"
    "ds_read_b64_tr_b16 v[28:29], v2 offset:12288\n"
    "v_mfma_f32_16x16x32_bf16 v[68:71], v[16:19], v[140:143], v[68:71]\n"
    "ds_read_b64_tr_b16 v[30:31], v2 offset:12800\n"
    "s_waitcnt lgkmcnt(6)\n"
    "v_mfma_f32_16x16x32_bf16 v[76:79], v[20:23], v[140:143], v[76:79]\n"
    "ds_read_b64_tr_b16 v[16:17], v1 offset:16384\n"
    "v_mfma_f32_16x16x32_bf16 v[72:75], v[20:23], v[136:139], v[72:75]\n"
    "ds_read_b64_tr_b16 v[18:19], v1 offset:16896\n"
    "s_waitcnt lgkmcnt(5)\n"
    "v_mfma_f32_16x16x32_bf16 v[80:83], v[24:27], v[136:139], v[80:83]\n"
    "ds_read_b64_tr_b16 v[20:21], v2 offset:16384\n"
    "v_mfma_f32_16x16x32_bf16 v[84:87], v[24:27], v[140:143], v[84:87]\n"
    "ds_read_b64_tr_b16 v[22:23], v2 offset:16896\n"
    "s_waitcnt lgkmcnt(4)\n"
    "v_mfma_f32_16x16x32_bf16 v[92:95], v[28:31], v[140:143], v[92:95]\n"
    "ds_read_b64_tr_b16 v[24:25], v1 offset:24576\n"
    "v_mfma_f32_16x16x32_bf16 v[88:91], v[28:31], v[136:139], v[88:91]\n"
    "ds_read_b64_tr_b16 v[26:27], v1 offset:25088\n"
    "s_waitcnt vmcnt(4)\n"
    "s_cbranch_vccz .Lfz_RET_bX3\n"
    "s_barrier\n"
    ".Lfz_RET_bX3:\n"
    "s_lshl_b32 s87, %2, 3\n"
    "s_add_u32 s87, s87, s48\n"
    "s_mul_i32 s94, s87, 896\n"
    "v_lshlrev_b32 v112, 7, v0\n"
    "s_mov_b64 exec, 0x7f\n"
    "buffer_load_dword v113, v112, s[36:39], s94 offen\n"
    "s_mov_b64 exec, 3\n"
    "s_lshl_b32 s94, s87, 8\n"
    "buffer_load_dword v114, v112, s[24:27], s94 offen\n"
    "s_mov_b64 exec, -1\n"
    "v_cndmask_b32 v48, v34, 0, s[4:5]\n"
    "v_cndmask_b32 v49, v35, 0, s[4:5]\n"
    "v_cndmask_b32 v50, 0, v34, s[4:5]\n"
    "v_cndmask_b32 v51, 0, v35, s[4:5]\n"
    "v_cndmask_b32 v52, v36, 0, s[4:5]\n"
    "v_cndmask_b32 v53, v37, 0, s[4:5]\n"
    "v_cndmask_b32 v54, 0, v36, s[4:5]\n"
    "v_cndmask_b32 v55, 0, v37, s[4:5]\n"
    "v_cndmask_b32 v56, v38, 0, s[4:5]\n"
    "v_cndmask_b32 v57, v39, 0, s[4:5]\n"
    "v_cndmask_b32 v58, 0, v38, s[4:5]\n"
    "s_cbranch_vccnz .Lfz_RET_bX4\n"
    "s_barrier\n"
    ".Lfz_RET_bX4:\n"
    "s_waitcnt lgkmcnt(4)\n"
    "v_mfma_f32_16x16x32_bf16 v[64:67], v[16:19], v[144:147], v[64:67]\n"
    "ds_read_b64_tr_b16 v[28:29], v2 offset:24576\n"
    "v_mfma_f32_16x16x32_bf16 v[68:71], v[16:19], v[148:151], v[68:71]\n"
    "ds_read_b64_tr_b16 v[30:31], v2 offset:25088\n"
    "ds_read_b128 v[160:163], v4 offset:32768\n"
    "s_waitcnt lgkmcnt(5)\n"
    "v_mfma_f32_16x16x32_bf16 v[76:79], v[20:23], v[148:151], v[76:79]\n"
    "ds_read_b64_tr_b16 v[16:17], v1 offset:20480\n"
    "v_mfma_f32_16x16x32_bf16 v[72:75], v[20:23], v[144:147], v[72:75]\n"
    "ds_read_b64_tr_b16 v[18:19], v1 offset:20992\n"
    "ds_read_b128 v[164:167], v4 offset:34816\n"
    "s_waitcnt lgkmcnt(6)\n"
    "v_mfma_f32_16x16x32_bf16 v[80:83], v[24:27], v[144:147], v[80:83]\n"
    "ds_read_b64_tr_b16 v[20:21], v2 offset:20480\n"
    "v_mfma_f32_16x16x32_bf16 v[84:87], v[24:27], v[148:151], v[84:87]\n"
    "ds_read_b64_tr_b16 v[22:23], v2 offset:20992\n"
    "ds_read_b128 v[168:171], v5 offset:32768\n"
    "s_waitcnt lgkmcnt(7)\n"
    "v_mfma_f32_16x16x32_bf16 v[92:95], v[28:31], v[148:151], v[92:95]\n"
    "ds_read_b64_tr_b16 v[24:25], v1 offset:28672\n"
    "v_mfma_f32_16x16x32_bf16 v[88:91], v[28:31], v[144:147], v[88:91]\n"
    "ds_read_b64_tr_b16 v[26:27], v1 offset:29184\n"
    "ds_read_b128 v[172:175], v5 offset:34816\n"
    "s_waitcnt lgkmcnt(7)\n"
    "v_mfma_f32_16x16x32_bf16 v[64:67], v[16:19], v[152:155], v[64:67]\n"
    "ds_read_b64_tr_b16 v[28:29], v2 offset:28672\n"
    "v_mfma_f32_16x16x32_bf16 v[68:71], v[16:19], v[156:159], v[68:71]\n"
    "ds_read_b64_tr_b16 v[30:31], v2 offset:29184\n"
    "s_waitcnt lgkmcnt(6)\n"
    "v_mfma_f32_16x16x32_bf16 v[76:79], v[20:23], v[156:159], v[76:79]\n"
    "ds_read_b64_tr_b16 v[16:17], v1 offset:32768\n"
    "v_mfma_f32_16x16x32_bf16 v[72:75], v[20:23], v[152:155], v[72:75]\n"
    "ds_read_b64_tr_b16 v[18:19], v1 offset:33280\n"
    "s_waitcnt lgkmcnt(5)\n"
    "v_mfma_f32_16x16x32_bf16 v[80:83], v[24:27], v[152:155], v[80:83]\n"
    "ds_read_b64_tr_b16 v[20:21], v2 offset:32768\n"
    "v_mfma_f32_16x16x32_bf16 v[84:87], v[24:27], v[156:159], v[84:87]\n"
    "ds_read_b64_tr_b16 v[22:23], v2 offset:33280\n"
    "s_waitcnt lgkmcnt(4)\n"
    "v_mfma_f32_16x16x32_bf16 v[92:95], v[28:31], v[156:159], v[92:95]\n"
    "ds_read_b64_tr_b16 v[24:25], v1 offset:40960\n"
    "v_mfma_f32_16x16x32_bf16 v[88:91], v[28:31], v[152:155], v[88:91]\n"
    "ds_read_b64_tr_b16 v[26:27], v1 offset:41472\n"
    "s_waitcnt vmcnt(4)\n"
    "s_cbranch_vccz .Lfz_RET_bX5\n"
    "s_barrier\n"
    ".Lfz_RET_bX5:\n"
    "s_add_u32 m0, s52, 0x0\n"
    "s_lshl_b32 s54, s61, 7\n"
    "s_add_u32 s54, s54, s86\n"
    "buffer_load_dwordx4 v32, s[44:47], s54 offen sc1 lds\n"
    "v_cndmask_b32 v59, 0, v39, s[4:5]\n"
    "v_cndmask_b32 v60, v40, 0, s[4:5]\n"
    "v_cndmask_b32 v61, v41, 0, s[4:5]\n"
    "v_cndmask_b32 v62, 0, v40, s[4:5]\n"
    "v_cndmask_b32 v63, 0, v41, s[4:5]\n"
    "s_add_u32 m0, s52, 0x2000\n"
    "s_lshl_b32 s55, s61, 7\n"
    "s_add_u32 s55, s55, s86\n"
    "s_add_u32 s55, s55, 0x10000\n"
    "buffer_load_dwordx4 v32, s[44:47], s55 offen sc1 lds\n"
    "s_add_u32 s76, s70, 8192\n"
    "s_lshl_b32 s87, s50, 4\n"
    "s_add_u32 s87, s87, s49\n"
    "s_mul_i32 s75, s87, 640\n"
    "s_add_u32 s75, s75, 0xb48000\n"
    "s_lshl_b32 s94, s48, 6\n"
    "s_add_u32 s74, s75, s94\n"
    "s_mul_i32 s87, s87, 18432\n"
    "s_add_u32 s87, s87, 0x908000\n"
    "s_lshl_b32 s94, s48, 7\n"
    "s_add_u32 s71, s87, s94\n"
    "s_add_u32 s72, s71, 9216\n"
    "s_add_u32 s87, s87, 1024\n"
    "s_cmp_eq_u32 s48, 0\n"
    "s_cselect_b32 s73, s87, 0x40000000\n"
    "s_cselect_b32 s75, s75, 0x40000000\n"
    "s_cselect_b32 s79, s53, 0\n"
    "s_add_u32 m0, s52, 0x14000\n"
    "s_mul_i32 s54, s61, 0x38000\n"
    "buffer_load_dwordx4 v6, s[28:31], s54 offen lds\n"
    "s_add_u32 m0, s52, 0x16000\n"
    "s_mul_i32 s55, s61, 0x38000\n"
    "buffer_load_dwordx4 v6, s[32:35], s55 offen lds\n"
    "s_add_u32 s68, s67, 32768\n"
    "s_add_u32 s69, s67, 114688\n"
    "v_lshrrev_b32 v34, 3, v0\n"
    "v_mul_u32_u24 v34, 1152, v34\n"
    "s_cbranch_vccnz .Lfz_RET_bX6\n"
    "s_barrier\n"
    ".Lfz_RET_bX6:\n"
    "s_waitcnt lgkmcnt(4)\n"
    "v_mfma_f32_16x16x32_bf16 v[64:67], v[16:19], v[160:163], v[64:67]\n"
    "ds_read_b64_tr_b16 v[28:29], v2 offset:40960\n"
    "v_mfma_f32_16x16x32_bf16 v[68:71], v[16:19], v[164:167], v[68:71]\n"
    "ds_read_b64_tr_b16 v[30:31], v2 offset:41472\n"
    "ds_read_b128 v[176:179], v4 offset:49152\n"
    "s_waitcnt lgkmcnt(5)\n"
    "v_mfma_f32_16x16x32_bf16 v[76:79], v[20:23], v[164:167], v[76:79]\n"
    "ds_read_b64_tr_b16 v[16:17], v1 offset:36864\n"
    "v_mfma_f32_16x16x32_bf16 v[72:75], v[20:23], v[160:163], v[72:75]\n"
    "ds_read_b64_tr_b16 v[18:19], v1 offset:37376\n"
    "ds_read_b128 v[180:183], v4 offset:51200\n"
    "s_waitcnt lgkmcnt(6)\n"
    "v_mfma_f32_16x16x32_bf16 v[80:83], v[24:27], v[160:163], v[80:83]\n"
    "ds_read_b64_tr_b16 v[20:21], v2 offset:36864\n"
    "v_mfma_f32_16x16x32_bf16 v[84:87], v[24:27], v[164:167], v[84:87]\n"
    "ds_read_b64_tr_b16 v[22:23], v2 offset:37376\n"
    "ds_read_b128 v[184:187], v5 offset:49152\n"
    "s_waitcnt lgkmcnt(7)\n"
    "v_mfma_f32_16x16x32_bf16 v[92:95], v[28:31], v[164:167], v[92:95]\n"
    "ds_read_b64_tr_b16 v[24:25], v1 offset:45056\n"
    "v_mfma_f32_16x16x32_bf16 v[88:91], v[28:31], v[160:163], v[88:91]\n"
    "ds_read_b64_tr_b16 v[26:27], v1 offset:45568\n"
    "ds_read_b128 v[188:191], v5 offset:51200\n"
    "s_waitcnt lgkmcnt(7)\n"
    "v_mfma_f32_16x16x32_bf16 v[64:67], v[16:19], v[168:171], v[64:67]\n"
    "ds_read_b64_tr_b16 v[28:29], v2 offset:45056\n"
    "v_mfma_f32_16x16x32_bf16 v[68:71], v[16:19], v[172:175], v[68:71]\n"
    "ds_read_b64_tr_b16 v[30:31], v2 offset:45568\n"
    "s_waitcnt lgkmcnt(6)\n"
    "v_mfma_f32_16x16x32_bf16 v[76:79], v[20:23], v[172:175], v[76:79]\n"
    "ds_read_b64_tr_b16 v[16:17], v1 offset:49152\n"
    "v_mfma_f32_16x16x32_bf16 v[72:75], v[20:23], v[168:171], v[72:75]\n"
    "ds_read_b64_tr_b16 v[18:19], v1 offset:49664\n"
    "s_waitcnt lgkmcnt(5)\n"
    "v_mfma_f32_16x16x32_bf16 v[80:83], v[24:27], v[168:171], v[80:83]\n"
    "ds_read_b64_tr_b16 v[20:21], v2 offset:49152\n"
    "v_mfma_f32_16x16x32_bf16 v[84:87], v[24:27], v[172:175], v[84:87]\n"
    "ds_read_b64_tr_b16 v[22:23], v2 offset:49664\n"
    "s_waitcnt lgkmcnt(4)\n"
    "v_mfma_f32_16x16x32_bf16 v[92:95], v[28:31], v[172:175], v[92:95]\n"
    "ds_read_b64_tr_b16 v[24:25], v1 offset:57344\n"
    "v_mfma_f32_16x16x32_bf16 v[88:91], v[28:31], v[168:171], v[88:91]\n"
    "ds_read_b64_tr_b16 v[26:27], v1 offset:57856\n"
    "s_waitcnt vmcnt(6)\n"
    "s_cbranch_vccz .Lfz_RET_bX7\n"
    "s_barrier\n"
    ".Lfz_RET_bX7:\n"
    "s_add_u32 m0, s52, 0x4000\n"
    "s_lshl_b32 s54, s62, 7\n"
    "s_add_u32 s54, s54, s86\n"
    "buffer_load_dwordx4 v32, s[44:47], s54 offen sc1 lds\n"
    "v_and_b32 v35, 7, v0\n"
    "v_lshl_add_u32 v41, v35, 4, v34\n"
    "v_lshrrev_b32 v34, 2, v0\n"
    "v_mul_u32_u24 v34, 1152, v34\n"
    "v_and_b32 v35, 3, v0\n"
    "s_add_u32 m0, s52, 0x6000\n"
    "s_lshl_b32 s55, s62, 7\n"
    "s_add_u32 s55, s55, s86\n"
    "s_add_u32 s55, s55, 0x10000\n"
    "buffer_load_dwordx4 v32, s[44:47], s55 offen sc1 lds\n"
    "v_lshl_add_u32 v42, v35, 4, v34\n"
    "v_lshlrev_b32 v43, 3, v0\n"
    "v_lshrrev_b32 v34, 5, v0\n"
    "v_and_b32 v35, 31, v0\n"
    "s_add_u32 m0, s52, 0x18000\n"
    "s_mul_i32 s54, s62, 0x38000\n"
    "buffer_load_dwordx4 v6, s[28:31], s54 offen lds\n"
    "v_mul_u32_u24 v44, 640, v34\n"
    "v_lshl_add_u32 v44, v35, 4, v44\n"
    "v_mul_u32_u24 v34, 288, v34\n"
    "s_mul_i32 s87, s48, 576\n"
    "v_add_u32 v34, s87, v34\n"
    "s_add_u32 m0, s52, 0x1a000\n"
    "s_mul_i32 s55, s62, 0x38000\n"
    "buffer_load_dwordx4 v6, s[32:35], s55 offen lds\n"
    "v_lshl_add_u32 v45, v35, 3, v34\n"
    "v_and_b32 v35, 15, v0\n"
    "v_lshl_add_u32 v34, v35, 4, v34\n"
    "s_cbranch_vccnz .Lfz_RET_bX8\n"
    "s_barrier\n"
    ".Lfz_RET_bX8:\n"
    "s_waitcnt lgkmcnt(4)\n"
    "v_mfma_f32_16x16x32_bf16 v[64:67], v[16:19], v[176:179], v[64:67]\n"
    "ds_read_b64_tr_b16 v[28:29], v2 offset:57344\n"
    "v_mfma_f32_16x16x32_bf16 v[68:71], v[16:19], v[180:183], v[68:71]\n"
    "ds_read_b64_tr_b16 v[30:31], v2 offset:57856\n"
    "v_add_u32 v4, 0x10000, v4\n"
    "ds_read_b128 v[192:195], v4 offset:0\n"
    "s_waitcnt lgkmcnt(5)\n"
    "v_mfma_f32_16x16x32_bf16 v[76:79], v[20:23], v[180:183], v[76:79]\n"
    "ds_read_b64_tr_b16 v[16:17], v1 offset:53248\n"
    "v_mfma_f32_16x16x32_bf16 v[72:75], v[20:23], v[176:179], v[72:75]\n"
    "ds_read_b64_tr_b16 v[18:19], v1 offset:53760\n"
    "ds_read_b128 v[196:199], v4 offset:2048\n"
    "s_waitcnt lgkmcnt(6)\n"
    "v_mfma_f32_16x16x32_bf16 v[80:83], v[24:27], v[176:179], v[80:83]\n"
    "ds_read_b64_tr_b16 v[20:21], v2 offset:53248\n"
    "v_mfma_f32_16x16x32_bf16 v[84:87], v[24:27], v[180:183], v[84:87]\n"
    "ds_read_b64_tr_b16 v[22:23], v2 offset:53760\n"
    "v_add_u32 v5, 0x10000, v5\n"
    "ds_read_b128 v[200:203], v5 offset:0\n"
    "s_waitcnt lgkmcnt(7)\n"
    "v_mfma_f32_16x16x32_bf16 v[92:95], v[28:31], v[180:183], v[92:95]\n"
    "ds_read_b64_tr_b16 v[24:25], v1 offset:61440\n"
    "v_mfma_f32_16x16x32_bf16 v[88:91], v[28:31], v[176:179], v[88:91]\n"
    "ds_read_b64_tr_b16 v[26:27], v1 offset:61952\n"
    "ds_read_b128 v[204:207], v5 offset:2048\n"
    "s_waitcnt lgkmcnt(7)\n"
    "v_mfma_f32_16x16x32_bf16 v[64:67], v[16:19], v[184:187], v[64:67]\n"
    "ds_read_b64_tr_b16 v[28:29], v2 offset:61440\n"
    "v_mfma_f32_16x16x32_bf16 v[68:71], v[16:19], v[188:191], v[68:71]\n"
    "ds_read_b64_tr_b16 v[30:31], v2 offset:61952\n"
    "s_waitcnt lgkmcnt(6)\n"
    "v_mfma_f32_16x16x32_bf16 v[76:79], v[20:23], v[188:191], v[76:79]\n"
    "v_add_u32 v1, 0x10000, v1\n"
    "ds_read_b64_tr_b16 v[16:17], v1 offset:0\n"
    "v_mfma_f32_16x16x32_bf16 v[72:75], v[20:23], v[184:187], v[72:75]\n"
    "ds_read_b64_tr_b16 v[18:19], v1 offset:512\n"
    "s_waitcnt lgkmcnt(5)\n"
    "v_mfma_f32_16x16x32_bf16 v[80:83], v[24:27], v[184:187], v[80:83]\n"
    "v_add_u32 v2, 0x10000, v2\n"
    "ds_read_b64_tr_b16 v[20:21], v2 offset:0\n"
    "v_mfma_f32_16x16x32_bf16 v[84:87], v[24:27], v[188:191], v[84:87]\n"
    "ds_read_b64_tr_b16 v[22:23], v2 offset:512\n"
    "s_waitcnt lgkmcnt(4)\n"
    "v_mfma_f32_16x16x32_bf16 v[92:95], v[28:31], v[188:191], v[92:95]\n"
    "ds_read_b64_tr_b16 v[24:25], v1 offset:8192\n"
    "v_mfma_f32_16x16x32_bf16 v[88:91], v[28:31], v[184:187], v[88:91]\n"
    "ds_read_b64_tr_b16 v[26:27], v1 offset:8704\n"
    "s_waitcnt vmcnt(4)\n"
    "s_cbranch_vccz .Lfz_RET_bX9\n"
    "s_barrier\n"
    ".Lfz_RET_bX9:\n"
    "s_add_u32 m0, s52, 0x8000\n"
    "s_lshl_b32 s54, s63, 7\n"
    "s_add_u32 s54, s54, s86\n"
    "buffer_load_dwordx4 v32, s[44:47], s54 offen sc1 lds\n"
    "v_bfe_u32 v36, v0, 4, 1\n"
    "v_lshl_add_u32 v34, v36, 2, v34\n"
    "v_lshl_or_b32 v45, v45, 16, v34\n"
    "v_lshrrev_b32 v34, 4, v0\n"
    "s_add_u32 m0, s52, 0xa000\n"
    "s_lshl_b32 s55, s63, 7\n"
    "s_add_u32 s55, s55, s86\n"
    "s_add_u32 s55, s55, 0x10000\n"
    "buffer_load_dwordx4 v32, s[44:47], s55 offen sc1 lds\n"
    "v_mul_u32_u24 v34, 576, v34\n"
    "s_mul_i32 s87, s84, 2304\n"
    "v_add_u32 v34, s87, v34\n"
    "v_lshl_add_u32 v35, v35, 4, v34\n"
    "v_add_u32 v36, 0x110, v34\n"
    "s_add_u32 m0, s52, 0x1c000\n"
    "s_mul_i32 s54, s63, 0x38000\n"
    "buffer_load_dwordx4 v6, s[28:31], s54 offen lds\n"
    "v_add_u32 v34, 0x100, v34\n"
    "s_mov_b32 s90, -1\n"
    "s_mov_b32 s91, 0\n"
    "v_cndmask_b32 v37, v36, v35, s[90:91]\n"
    "v_cndmask_b32 v47, v35, v36, s[90:91]\n"
    "s_mov_b32 s90, 0x11111111\n"
    "s_mov_b32 s91, 0x22222222\n"
    "v_cndmask_b32 v46, v36, v34, s[90:91]\n"
    "s_add_u32 m0, s52, 0x1e000\n"
    "s_mul_i32 s55, s63, 0x38000\n"
    "buffer_load_dwordx4 v6, s[32:35], s55 offen lds\n"
    "v_lshl_or_b32 v47, v47, 16, v37\n"
    "s_cbranch_vccnz .Lfz_RET_bX10\n"
    "s_barrier\n"
    ".Lfz_RET_bX10:\n"
    "s_waitcnt lgkmcnt(4)\n"
    "v_mfma_f32_16x16x32_bf16 v[64:67], v[16:19], v[192:195], v[64:67]\n"
    "ds_read_b64_tr_b16 v[28:29], v2 offset:8192\n"
    "v_mfma_f32_16x16x32_bf16 v[68:71], v[16:19], v[196:199], v[68:71]\n"
    "ds_read_b64_tr_b16 v[30:31], v2 offset:8704\n"
    "v_add_u32 v4, 0xfffdc000, v4\n"
    "ds_read_b128 v[208:211], v4 offset:0\n"
    "s_waitcnt lgkmcnt(5)\n"
    "v_mfma_f32_16x16x32_bf16 v[76:79], v[20:23], v[196:199], v[76:79]\n"
    "ds_read_b64_tr_b16 v[16:17], v1 offset:4096\n"
    "v_mfma_f32_16x16x32_bf16 v[72:75], v[20:23], v[192:195], v[72:75]\n"
    "ds_read_b64_tr_b16 v[18:19], v1 offset:4608\n"
    "ds_read_b128 v[212:215], v4 offset:2048\n"
    "s_waitcnt lgkmcnt(6)\n"
    "v_mfma_f32_16x16x32_bf16 v[80:83], v[24:27], v[192:195], v[80:83]\n"
    "ds_read_b64_tr_b16 v[20:21], v2 offset:4096\n"
    "v_mfma_f32_16x16x32_bf16 v[84:87], v[24:27], v[196:199], v[84:87]\n"
    "ds_read_b64_tr_b16 v[22:23], v2 offset:4608\n"
    "v_add_u32 v5, 0xfffdc000, v5\n"
    "ds_read_b128 v[216:219], v5 offset:0\n"
    "s_waitcnt lgkmcnt(7)\n"
    "v_mfma_f32_16x16x32_bf16 v[92:95], v[28:31], v[196:199], v[92:95]\n"
    "ds_read_b64_tr_b16 v[24:25], v1 offset:12288\n"
    "v_mfma_f32_16x16x32_bf16 v[88:91], v[28:31], v[192:195], v[88:91]\n"
    "ds_read_b64_tr_b16 v[26:27], v1 offset:12800\n"
    "ds_read_b128 v[220:223], v5 offset:2048\n"
    "s_waitcnt lgkmcnt(7)\n"
    "v_mfma_f32_16x16x32_bf16 v[64:67], v[16:19], v[200:203], v[64:67]\n"
    "ds_read_b64_tr_b16 v[28:29], v2 offset:12288\n"
    "v_mfma_f32_16x16x32_bf16 v[68:71], v[16:19], v[204:207], v[68:71]\n"
    "ds_read_b64_tr_b16 v[30:31], v2 offset:12800\n"
    "s_waitcnt lgkmcnt(6)\n"
    "v_mfma_f32_16x16x32_bf16 v[76:79], v[20:23], v[204:207], v[76:79]\n"
    "ds_read_b64_tr_b16 v[16:17], v1 offset:16384\n"
    "v_mfma_f32_16x16x32_bf16 v[72:75], v[20:23], v[200:203], v[72:75]\n"
    "ds_read_b64_tr_b16 v[18:19], v1 offset:16896\n"
    "s_waitcnt lgkmcnt(5)\n"
    "v_mfma_f32_16x16x32_bf16 v[80:83], v[24:27], v[200:203], v[80:83]\n"
    "ds_read_b64_tr_b16 v[20:21], v2 offset:16384\n"
    "v_mfma_f32_16x16x32_bf16 v[84:87], v[24:27], v[204:207], v[84:87]\n"
    "ds_read_b64_tr_b16 v[22:23], v2 offset:16896\n"
    "s_waitcnt lgkmcnt(4)\n"
    "v_mfma_f32_16x16x32_bf16 v[92:95], v[28:31], v[204:207], v[92:95]\n"
    "ds_read_b64_tr_b16 v[24:25], v1 offset:24576\n"
    "v_mfma_f32_16x16x32_bf16 v[88:91], v[28:31], v[200:203], v[88:91]\n"
    "ds_read_b64_tr_b16 v[26:27], v1 offset:25088\n"
    "s_waitcnt vmcnt(4)\n"
    "s_cbranch_vccz .Lfz_RET_bX11\n"
    "s_barrier\n"
    ".Lfz_RET_bX11:\n"
    "s_add_u32 m0, s52, 0x20000\n"
    "s_mul_i32 s54, s56, 0x38000\n"
    "s_add_u32 s54, s54, 0x400\n"
    "buffer_load_dwordx4 v6, s[28:31], s54 offen lds\n"
    "s_add_u32 m0, s52, 0x22000\n"
    "s_mul_i32 s55, s56, 0x38000\n"
    "s_add_u32 s55, s55, 0x400\n"
    "buffer_load_dwordx4 v6, s[32:35], s55 offen lds\n"
    "s_add_u32 m0, s52, 0x24000\n"
    "s_mul_i32 s54, s57, 0x38000\n"
    "s_add_u32 s54, s54, 0x400\n"
    "buffer_load_dwordx4 v6, s[28:31], s54 offen lds\n"
    "s_add_u32 m0, s52, 0x26000\n"
    "s_mul_i32 s55, s57, 0x38000\n"
    "s_add_u32 s55, s55, 0x400\n"
    "buffer_load_dwordx4 v6, s[32:35], s55 offen lds\n"
    "s_cbranch_vccnz .Lfz_RET_bX12\n"
    "s_barrier\n"
    ".Lfz_RET_bX12:\n"
    "s_waitcnt lgkmcnt(4)\n"
    "v_mfma_f32_16x16x32_bf16 v[64:67], v[16:19], v[208:211], v[64:67]\n"
    "ds_read_b64_tr_b16 v[28:29], v2 offset:24576\n"
    "v_mfma_f32_16x16x32_bf16 v[68:71], v[16:19], v[212:215], v[68:71]\n"
    "ds_read_b64_tr_b16 v[30:31], v2 offset:25088\n"
    "ds_read_b128 v[224:227], v4 offset:16384\n"
    "s_waitcnt lgkmcnt(5)\n"
    "v_mfma_f32_16x16x32_bf16 v[76:79], v[20:23], v[212:215], v[76:79]\n"
    "ds_read_b64_tr_b16 v[16:17], v1 offset:20480\n"
    "v_mfma_f32_16x16x32_bf16 v[72:75], v[20:23], v[208:211], v[72:75]\n"
    "ds_read_b64_tr_b16 v[18:19], v1 offset:20992\n"
    "ds_read_b128 v[228:231], v4 offset:18432\n"
    "s_waitcnt lgkmcnt(6)\n"
    "v_mfma_f32_16x16x32_bf16 v[80:83], v[24:27], v[208:211], v[80:83]\n"
    "ds_read_b64_tr_b16 v[20:21], v2 offset:20480\n"
    "v_mfma_f32_16x16x32_bf16 v[84:87], v[24:27], v[212:215], v[84:87]\n"
    "ds_read_b64_tr_b16 v[22:23], v2 offset:20992\n"
    "ds_read_b128 v[232:235], v5 offset:16384\n"
    "s_waitcnt lgkmcnt(7)\n"
    "v_mfma_f32_16x16x32_bf16 v[92:95], v[28:31], v[212:215], v[92:95]\n"
    "ds_read_b64_tr_b16 v[24:25], v1 offset:28672\n"
    "v_mfma_f32_16x16x32_bf16 v[88:91], v[28:31], v[208:211], v[88:91]\n"
    "ds_read_b64_tr_b16 v[26:27], v1 offset:29184\n"
    "ds_read_b128 v[236:239], v5 offset:18432\n"
    "s_waitcnt lgkmcnt(7)\n"
    "v_mfma_f32_16x16x32_bf16 v[64:67], v[16:19], v[216:219], v[64:67]\n"
    "ds_read_b64_tr_b16 v[28:29], v2 offset:28672\n"
    "v_mfma_f32_16x16x32_bf16 v[68:71], v[16:19], v[220:223], v[68:71]\n"
    "ds_read_b64_tr_b16 v[30:31], v2 offset:29184\n"
    "s_waitcnt lgkmcnt(6)\n"
    "v_mfma_f32_16x16x32_bf16 v[76:79], v[20:23], v[220:223], v[76:79]\n"
    "ds_read_b64_tr_b16 v[16:17], v1 offset:32768\n"
    "v_mfma_f32_16x16x32_bf16 v[72:75], v[20:23], v[216:219], v[72:75]\n"
    "ds_read_b64_tr_b16 v[18:19], v1 offset:33280\n"
    "s_waitcnt lgkmcnt(5)\n"
    "v_mfma_f32_16x16x32_bf16 v[80:83], v[24:27], v[216:219], v[80:83]\n"
    "ds_read_b64_tr_b16 v[20:21], v2 offset:32768\n"
    "v_mfma_f32_16x16x32_bf16 v[84:87], v[24:27], v[220:223], v[84:87]\n"
    "ds_read_b64_tr_b16 v[22:23], v2 offset:33280\n"
    "s_waitcnt lgkmcnt(4)\n"
    "v_mfma_f32_16x16x32_bf16 v[92:95], v[28:31], v[220:223], v[92:95]\n"
    "ds_read_b64_tr_b16 v[24:25], v1 offset:40960\n"
    "v_mfma_f32_16x16x32_bf16 v[88:91], v[28:31], v[216:219], v[88:91]\n"
    "ds_read_b64_tr_b16 v[26:27], v1 offset:41472\n"
    "s_waitcnt vmcnt(4)\n"
    "s_cbranch_vccz .Lfz_RET_bX13\n"
    "s_barrier\n"
    ".Lfz_RET_bX13:\n"
    "s_add_u32 m0, s52, 0x0\n"
    "s_mul_i32 s54, s58, 0x38000\n"
    "s_add_u32 s54, s54, 0x400\n"
    "buffer_load_dwordx4 v6, s[28:31], s54 offen lds\n"
    "s_add_u32 m0, s52, 0x2000\n"
    "s_mul_i32 s55, s58, 0x38000\n"
    "s_add_u32 s55, s55, 0x400\n"
    "buffer_load_dwordx4 v6, s[32:35], s55 offen lds\n"
    "s_add_u32 m0, s52, 0xc000\n"
    "s_mul_i32 s54, s59, 0x38000\n"
    "s_add_u32 s54, s54, 0x400\n"
    "buffer_load_dwordx4 v6, s[28:31], s54 offen lds\n"
    "s_add_u32 m0, s52, 0xe000\n"
    "s_mul_i32 s55, s59, 0x38000\n"
    "s_add_u32 s55, s55, 0x400\n"
    "buffer_load_dwordx4 v6, s[32:35], s55 offen lds\n"
    "s_cbranch_vccnz .Lfz_RET_bX14\n"
    "s_barrier\n"
    ".Lfz_RET_bX14:\n"
    "s_waitcnt lgkmcnt(4)\n"
    "v_mfma_f32_16x16x32_bf16 v[64:67], v[16:19], v[224:227], v[64:67]\n"
    "ds_read_b64_tr_b16 v[28:29], v2 offset:40960\n"
    "v_mfma_f32_16x16x32_bf16 v[68:71], v[16:19], v[228:231], v[68:71]\n"
    "ds_read_b64_tr_b16 v[30:31], v2 offset:41472\n"
    "ds_read_b128 v[240:243], v4 offset:32768\n"
    "s_waitcnt lgkmcnt(5)\n"
    "v_mfma_f32_16x16x32_bf16 v[76:79], v[20:23], v[228:231], v[76:79]\n"
    "ds_read_b64_tr_b16 v[16:17], v1 offset:36864\n"
    "v_mfma_f32_16x16x32_bf16 v[72:75], v[20:23], v[224:227], v[72:75]\n"
    "ds_read_b64_tr_b16 v[18:19], v1 offset:37376\n"
    "ds_read_b128 v[244:247], v4 offset:34816\n"
    "s_waitcnt lgkmcnt(6)\n"
    "v_mfma_f32_16x16x32_bf16 v[80:83], v[24:27], v[224:227], v[80:83]\n"
    "ds_read_b64_tr_b16 v[20:21], v2 offset:36864\n"
    "v_mfma_f32_16x16x32_bf16 v[84:87], v[24:27], v[228:231], v[84:87]\n"
    "ds_read_b64_tr_b16 v[22:23], v2 offset:37376\n"
    "ds_read_b128 v[248:251], v5 offset:32768\n"
    "s_waitcnt lgkmcnt(7)\n"
    "v_mfma_f32_16x16x32_bf16 v[92:95], v[28:31], v[228:231], v[92:95]\n"
    "ds_read_b64_tr_b16 v[24:25], v1 offset:45056\n"
    "v_mfma_f32_16x16x32_bf16 v[88:91], v[28:31], v[224:227], v[88:91]\n"
    "ds_read_b64_tr_b16 v[26:27], v1 offset:45568\n"
    "ds_read_b128 v[252:255], v5 offset:34816\n"
    "s_waitcnt lgkmcnt(7)\n"
    "v_mfma_f32_16x16x32_bf16 v[64:67], v[16:19], v[232:235], v[64:67]\n"
    "ds_read_b64_tr_b16 v[28:29], v2 offset:45056\n"
    "v_mfma_f32_16x16x32_bf16 v[68:71], v[16:19], v[236:239], v[68:71]\n"
    "ds_read_b64_tr_b16 v[30:31], v2 offset:45568\n"
    "s_waitcnt lgkmcnt(6)\n"
    "v_mfma_f32_16x16x32_bf16 v[76:79], v[20:23], v[236:239], v[76:79]\n"
    "ds_read_b64_tr_b16 v[16:17], v1 offset:49152\n"
    "v_mfma_f32_16x16x32_bf16 v[72:75], v[20:23], v[232:235], v[72:75]\n"
    "ds_read_b64_tr_b16 v[18:19], v1 offset:49664\n"
    "s_waitcnt lgkmcnt(5)\n"
    "v_mfma_f32_16x16x32_bf16 v[80:83], v[24:27], v[232:235], v[80:83]\n"
    "ds_read_b64_tr_b16 v[20:21], v2 offset:49152\n"
    "v_mfma_f32_16x16x32_bf16 v[84:87], v[24:27], v[236:239], v[84:87]\n"
    "ds_read_b64_tr_b16 v[22:23], v2 offset:49664\n"
    "s_waitcnt lgkmcnt(4)\n"
    "v_mfma_f32_16x16x32_bf16 v[92:95], v[28:31], v[236:239], v[92:95]\n"
    "ds_read_b64_tr_b16 v[24:25], v1 offset:57344\n"
    "v_mfma_f32_16x16x32_bf16 v[88:91], v[28:31], v[232:235], v[88:91]\n"
    "ds_read_b64_tr_b16 v[26:27], v1 offset:57856\n"
    "s_waitcnt vmcnt(6)\n"
    "s_cbranch_vccz .Lfz_RET_bX15\n"
    "s_barrier\n"
    ".Lfz_RET_bX15:\n"
    "s_add_u32 m0, s52, 0x10000\n"
    "s_mul_i32 s54, s60, 0x38000\n"
    "s_add_u32 s54, s54, 0x400\n"
    "buffer_load_dwordx4 v6, s[28:31], s54 offen lds\n"
    "s_add_u32 m0, s52, 0x12000\n"
    "s_mul_i32 s55, s60, 0x38000\n"
    "s_add_u32 s55, s55, 0x400\n"
    "buffer_load_dwordx4 v6, s[32:35], s55 offen lds\n"
    "s_add_u32 m0, s52, 0x14000\n"
    "s_mul_i32 s54, s61, 0x38000\n"
    "s_add_u32 s54, s54, 0x400\n"
    "buffer_load_dwordx4 v6, s[28:31], s54 offen lds\n"
    "s_add_u32 m0, s52, 0x16000\n"
    "s_mul_i32 s55, s61, 0x38000\n"
    "s_add_u32 s55, s55, 0x400\n"
    "buffer_load_dwordx4 v6, s[32:35], s55 offen lds\n"
    "s_cbranch_vccnz .Lfz_RET_bX16\n"
    "s_barrier\n"
    ".Lfz_RET_bX16:\n"
    "s_waitcnt lgkmcnt(4)\n"
    "v_mfma_f32_16x16x32_bf16 v[64:67], v[16:19], v[240:243], v[64:67]\n"
    "ds_read_b64_tr_b16 v[28:29], v2 offset:57344\n"
    "v_mfma_f32_16x16x32_bf16 v[68:71], v[16:19], v[244:247], v[68:71]\n"
    "ds_read_b64_tr_b16 v[30:31], v2 offset:57856\n"
    "s_waitcnt lgkmcnt(4)\n"
    "v_mfma_f32_16x16x32_bf16 v[76:79], v[20:23], v[244:247], v[76:79]\n"
    "ds_read_b64_tr_b16 v[16:17], v1 offset:53248\n"
    "v_mfma_f32_16x16x32_bf16 v[72:75], v[20:23], v[240:243], v[72:75]\n"
    "ds_read_b64_tr_b16 v[18:19], v1 offset:53760\n"
    "s_waitcnt lgkmcnt(4)\n"
    "v_mfma_f32_16x16x32_bf16 v[80:83], v[24:27], v[240:243], v[80:83]\n"
    "ds_read_b64_tr_b16 v[20:21], v2 offset:53248\n"
    "v_mfma_f32_16x16x32_bf16 v[84:87], v[24:27], v[244:247], v[84:87]\n"
    "ds_read_b64_tr_b16 v[22:23], v2 offset:53760\n"
    "s_waitcnt lgkmcnt(4)\n"
    "v_mfma_f32_16x16x32_bf16 v[92:95], v[28:31], v[244:247], v[92:95]\n"
    "ds_read_b64_tr_b16 v[24:25], v1 offset:61440\n"
    "v_mfma_f32_16x16x32_bf16 v[88:91], v[28:31], v[240:243], v[88:91]\n"
    "ds_read_b64_tr_b16 v[26:27], v1 offset:61952\n"
    "s_waitcnt lgkmcnt(4)\n"
    "v_mfma_f32_16x16x32_bf16 v[64:67], v[16:19], v[248:251], v[64:67]\n"
    "ds_read_b64_tr_b16 v[28:29], v2 offset:61440\n"
    "v_mfma_f32_16x16x32_bf16 v[68:71], v[16:19], v[252:255], v[68:71]\n"
    "ds_read_b64_tr_b16 v[30:31], v2 offset:61952\n"
    "s_waitcnt lgkmcnt(4)\n"
    "v_mfma_f32_16x16x32_bf16 v[76:79], v[20:23], v[252:255], v[76:79]\n"
    "v_add_u32 v1, 0x10000, v1\n"
    "ds_read_b64_tr_b16 v[16:17], v1 offset:0\n"
    "v_mfma_f32_16x16x32_bf16 v[72:75], v[20:23], v[248:251], v[72:75]\n"
    "ds_read_b64_tr_b16 v[18:19], v1 offset:512\n"
    "s_waitcnt lgkmcnt(4)\n"
    "v_mfma_f32_16x16x32_bf16 v[80:83], v[24:27], v[248:251], v[80:83]\n"
    "v_add_u32 v2, 0x10000, v2\n"
    "ds_read_b64_tr_b16 v[20:21], v2 offset:0\n"
    "v_mfma_f32_16x16x32_bf16 v[84:87], v[24:27], v[252:255], v[84:87]\n"
    "ds_read_b64_tr_b16 v[22:23], v2 offset:512\n"
    "s_waitcnt lgkmcnt(4)\n"
    "v_mfma_f32_16x16x32_bf16 v[92:95], v[28:31], v[252:255], v[92:95]\n"
    "ds_read_b64_tr_b16 v[24:25], v1 offset:8192\n"
    "v_mfma_f32_16x16x32_bf16 v[88:91], v[28:31], v[248:251], v[88:91]\n"
    "ds_read_b64_tr_b16 v[26:27], v1 offset:8704\n"
    "s_waitcnt vmcnt(8)\n"
    "s_cbranch_vccz .Lfz_RET_bX17\n"
    "s_barrier\n"
    ".Lfz_RET_bX17:\n"
    "s_add_u32 m0, s52, 0x18000\n"
    "s_mul_i32 s54, s62, 0x38000\n"
    "s_add_u32 s54, s54, 0x400\n"
    "buffer_load_dwordx4 v6, s[28:31], s54 offen lds\n"
    "v_mov_b32 v40, 0xbdd2d3e8\n"
    "v_mul_f32 v32, v64, v64\n"
    "v_mul_f32 v33, v65, v65\n"
    "v_mul_f32 v34, v66, v66\n"
    "v_mul_f32 v35, v67, v67\n"
    "v_fmaak_f32 v32, v40, v32, 0xc0135761\n"
    "s_add_u32 m0, s52, 0x1a000\n"
    "s_mul_i32 s55, s62, 0x38000\n"
    "s_add_u32 s55, s55, 0x400\n"
    "buffer_load_dwordx4 v6, s[32:35], s55 offen lds\n"
    "v_fmaak_f32 v33, v40, v33, 0xc0135761\n"
    "v_fmaak_f32 v34, v40, v34, 0xc0135761\n"
    "v_fmaak_f32 v35, v40, v35, 0xc0135761\n"
    "v_mul_f32 v32, v32, v64\n"
    "v_mul_f32 v33, v33, v65\n"
    "v_mul_f32 v34, v34, v66\n"
    "s_add_u32 m0, s52, 0x4000\n"
    "s_mul_i32 s54, s63, 0x38000\n"
    "s_add_u32 s54, s54, 0x400\n"
    "buffer_load_dwordx4 v6, s[28:31], s54 offen lds\n"
    "v_mul_f32 v35, v35, v67\n"
    "v_exp_f32 v32, v32\n"
    "v_mul_f32 v80, v80, v64\n"
    "v_exp_f32 v33, v33\n"
    "s_add_u32 m0, s52, 0x6000\n"
    "s_mul_i32 s55, s63, 0x38000\n"
    "s_add_u32 s55, s55, 0x400\n"
    "buffer_load_dwordx4 v6, s[32:35], s55 offen lds\n"
    "v_mul_f32 v81, v81, v65\n"
    "v_exp_f32 v34, v34\n"
    "v_mul_f32 v82, v82, v66\n"
    "v_exp_f32 v35, v35\n"
    "s_cbranch_vccnz .Lfz_RET_bX18\n"
    "s_barrier\n"
    ".Lfz_RET_bX18:\n"
    "s_waitcnt lgkmcnt(4)\n"
    "v_mfma_f32_16x16x32_bf16 v[96:99], v[16:19], v[128:131], 0\n"
    "ds_read_b64_tr_b16 v[28:29], v2 offset:8192\n"
    "v_mfma_f32_16x16x32_bf16 v[100:103], v[16:19], v[132:135], 0\n"
    "ds_read_b64_tr_b16 v[30:31], v2 offset:8704\n"
    "s_waitcnt lgkmcnt(4)\n"
    "v_mfma_f32_16x16x32_bf16 v[108:111], v[20:23], v[132:135], 0\n"
    "ds_read_b64_tr_b16 v[16:17], v1 offset:4096\n"
    "v_mfma_f32_16x16x32_bf16 v[104:107], v[20:23], v[128:131], 0\n"
    "ds_read_b64_tr_b16 v[18:19], v1 offset:4608\n"
    "s_waitcnt lgkmcnt(4)\n"
    "v_mfma_f32_16x16x32_bf16 v[112:115], v[24:27], v[128:131], 0\n"
    "ds_read_b64_tr_b16 v[20:21], v2 offset:4096\n"
    "v_mfma_f32_16x16x32_bf16 v[116:119], v[24:27], v[132:135], 0\n"
    "ds_read_b64_tr_b16 v[22:23], v2 offset:4608\n"
    "s_waitcnt lgkmcnt(4)\n"
    "v_mfma_f32_16x16x32_bf16 v[124:127], v[28:31], v[132:135], 0\n"
    "ds_read_b64_tr_b16 v[24:25], v1 offset:12288\n"
    "v_mfma_f32_16x16x32_bf16 v[120:123], v[28:31], v[128:131], 0\n"
    "ds_read_b64_tr_b16 v[26:27], v1 offset:12800\n"
    "s_waitcnt lgkmcnt(4)\n"
    "v_mfma_f32_16x16x32_bf16 v[96:99], v[16:19], v[136:139], v[96:99]\n"
    "ds_read_b64_tr_b16 v[28:29], v2 offset:12288\n"
    "v_mfma_f32_16x16x32_bf16 v[100:103], v[16:19], v[140:143], v[100:103]\n"
    "ds_read_b64_tr_b16 v[30:31], v2 offset:12800\n"
    "s_waitcnt lgkmcnt(4)\n"
    "v_mfma_f32_16x16x32_bf16 v[108:111], v[20:23], v[140:143], v[108:111]\n"
    "ds_read_b64_tr_b16 v[16:17], v1 offset:16384\n"
    "v_mfma_f32_16x16x32_bf16 v[104:107], v[20:23], v[136:139], v[104:107]\n"
    "ds_read_b64_tr_b16 v[18:19], v1 offset:16896\n"
    "s_waitcnt lgkmcnt(4)\n"
    "v_mfma_f32_16x16x32_bf16 v[112:115], v[24:27], v[136:139], v[112:115]\n"
    "ds_read_b64_tr_b16 v[20:21], v2 offset:16384\n"
    "v_mfma_f32_16x16x32_bf16 v[116:119], v[24:27], v[140:143], v[116:119]\n"
    "ds_read_b64_tr_b16 v[22:23], v2 offset:16896\n"
    "s_waitcnt lgkmcnt(4)\n"
    "v_mfma_f32_16x16x32_bf16 v[124:127], v[28:31], v[140:143], v[124:127]\n"
    "ds_read_b64_tr_b16 v[24:25], v1 offset:24576\n"
    "v_mfma_f32_16x16x32_bf16 v[120:123], v[28:31], v[136:139], v[120:123]\n"
    "ds_read_b64_tr_b16 v[26:27], v1 offset:25088\n"
    "s_waitcnt vmcnt(10)\n"
    "s_cbranch_vccz .Lfz_RET_bX19\n"
    "s_barrier\n"
    ".Lfz_RET_bX19:\n"
    "s_add_u32 m0, s52, 0x8000\n"
    "s_mul_i32 s54, s56, 0x30000\n"
    "buffer_load_dwordx4 v7, s[16:19], s54 offen lds\n"
    "v_mul_f32 v83, v83, v67\n"
    "v_add_f32 v32, 1.0, v32\n"
    "v_add_f32 v33, 1.0, v33\n"
    "v_add_f32 v34, 1.0, v34\n"
    "v_add_f32 v35, 1.0, v35\n"
    "v_rcp_f32 v32, v32\n"
    "s_add_u32 m0, s52, 0xa000\n"
    "s_mul_i32 s55, s57, 0x30000\n"
    "buffer_load_dwordx4 v7, s[16:19], s55 offen lds\n"
    "v_rcp_f32 v33, v33\n"
    "v_rcp_f32 v34, v34\n"
    "v_rcp_f32 v35, v35\n"
    "s_add_u32 m0, s52, 0x1c000\n"
    "s_mul_i32 s54, s58, 0x30000\n"
    "buffer_load_dwordx4 v7, s[16:19], s54 offen lds\n"
    "v_mul_f32 v32, v32, v80\n"
    "v_mul_f32 v33, v33, v81\n"
    "v_mul_f32 v34, v34, v82\n"
    "v_mul_f32 v35, v35, v83\n"
    "v_cvt_pk_bf16_f32 v36, v32, v33\n"
    "s_add_u32 m0, s52, 0x1e000\n"
    "s_mul_i32 s55, s59, 0x30000\n"
    "buffer_load_dwordx4 v7, s[16:19], s55 offen lds\n"
    "v_cvt_pk_bf16_f32 v37, v34, v35\n"
    "v_mul_f32 v32, v72, v72\n"
    "v_mul_f32 v33, v73, v73\n"
    "v_mul_f32 v34, v74, v74\n"
    "v_mul_f32 v35, v75, v75\n"
    "v_fmaak_f32 v32, v40, v32, 0xc0135761\n"
    "s_cbranch_vccnz .Lfz_RET_bX20\n"
    "s_barrier\n"
    ".Lfz_RET_bX20:\n"
    "s_waitcnt lgkmcnt(4)\n"
    "v_mfma_f32_16x16x32_bf16 v[96:99], v[16:19], v[144:147], v[96:99]\n"
    "ds_read_b64_tr_b16 v[28:29], v2 offset:24576\n"
    "v_mfma_f32_16x16x32_bf16 v[100:103], v[16:19], v[148:151], v[100:103]\n"
    "ds_read_b64_tr_b16 v[30:31], v2 offset:25088\n"
    "s_waitcnt lgkmcnt(4)\n"
    "v_mfma_f32_16x16x32_bf16 v[108:111], v[20:23], v[148:151], v[108:111]\n"
    "ds_read_b64_tr_b16 v[16:17], v1 offset:20480\n"
    "v_mfma_f32_16x16x32_bf16 v[104:107], v[20:23], v[144:147], v[104:107]\n"
    "ds_read_b64_tr_b16 v[18:19], v1 offset:20992\n"
    "s_waitcnt lgkmcnt(4)\n"
    "v_mfma_f32_16x16x32_bf16 v[112:115], v[24:27], v[144:147], v[112:115]\n"
    "ds_read_b64_tr_b16 v[20:21], v2 offset:20480\n"
    "v_mfma_f32_16x16x32_bf16 v[116:119], v[24:27], v[148:151], v[116:119]\n"
    "ds_read_b64_tr_b16 v[22:23], v2 offset:20992\n"
    "s_waitcnt lgkmcnt(4)\n"
    "v_mfma_f32_16x16x32_bf16 v[124:127], v[28:31], v[148:151], v[124:127]\n"
    "ds_read_b64_tr_b16 v[24:25], v1 offset:28672\n"
    "v_mfma_f32_16x16x32_bf16 v[120:123], v[28:31], v[144:147], v[120:123]\n"
    "ds_read_b64_tr_b16 v[26:27], v1 offset:29184\n"
    "s_waitcnt lgkmcnt(4)\n"
    "v_mfma_f32_16x16x32_bf16 v[96:99], v[16:19], v[152:155], v[96:99]\n"
    "ds_read_b64_tr_b16 v[28:29], v2 offset:28672\n"
    "v_mfma_f32_16x16x32_bf16 v[100:103], v[16:19], v[156:159], v[100:103]\n"
    "ds_read_b64_tr_b16 v[30:31], v2 offset:29184\n"
    "s_waitcnt lgkmcnt(4)\n"
    "v_mfma_f32_16x16x32_bf16 v[108:111], v[20:23], v[156:159], v[108:111]\n"
    "v_add_u32 v1, 0xfffe0000, v1\n"
    "ds_read_b64_tr_b16 v[16:17], v1 offset:0\n"
    "v_mfma_f32_16x16x32_bf16 v[104:107], v[20:23], v[152:155], v[104:107]\n"
    "ds_read_b64_tr_b16 v[18:19], v1 offset:512\n"
    "s_waitcnt lgkmcnt(4)\n"
    "v_mfma_f32_16x16x32_bf16 v[112:115], v[24:27], v[152:155], v[112:115]\n"
    "v_add_u32 v2, 0xfffe0000, v2\n"
    "ds_read_b64_tr_b16 v[20:21], v2 offset:0\n"
    "v_mfma_f32_16x16x32_bf16 v[116:119], v[24:27], v[156:159], v[116:119]\n"
    "ds_read_b64_tr_b16 v[22:23], v2 offset:512\n"
    "s_waitcnt lgkmcnt(4)\n"
    "v_mfma_f32_16x16x32_bf16 v[124:127], v[28:31], v[156:159], v[124:127]\n"
    "ds_read_b64_tr_b16 v[24:25], v1 offset:8192\n"
    "v_mfma_f32_16x16x32_bf16 v[120:123], v[28:31], v[152:155], v[120:123]\n"
    "ds_read_b64_tr_b16 v[26:27], v1 offset:8704\n"
    "s_waitcnt vmcnt(12)\n"
    "s_cbranch_vccz .Lfz_RET_bX21\n"
    "s_barrier\n"
    ".Lfz_RET_bX21:\n"
    "s_add_u32 m0, s52, 0x20000\n"
    "s_mul_i32 s54, s60, 0x30000\n"
    "buffer_load_dwordx4 v7, s[16:19], s54 offen lds\n"
    "v_fmaak_f32 v33, v40, v33, 0xc0135761\n"
    "v_fmaak_f32 v34, v40, v34, 0xc0135761\n"
    "v_fmaak_f32 v35, v40, v35, 0xc0135761\n"
    "v_mul_f32 v32, v32, v72\n"
    "v_mul_f32 v33, v33, v73\n"
    "v_mul_f32 v34, v34, v74\n"
    "v_mul_f32 v35, v35, v75\n"
    "v_exp_f32 v32, v32\n"
    "v_mul_f32 v88, v88, v72\n"
    "v_exp_f32 v33, v33\n"
    "v_mul_f32 v89, v89, v73\n"
    "s_add_u32 m0, s52, 0x22000\n"
    "s_mul_i32 s55, s61, 0x30000\n"
    "buffer_load_dwordx4 v7, s[16:19], s55 offen lds\n"
    "v_exp_f32 v34, v34\n"
    "v_mul_f32 v90, v90, v74\n"
    "v_exp_f32 v35, v35\n"
    "v_mul_f32 v91, v91, v75\n"
    "v_add_f32 v32, 1.0, v32\n"
    "v_add_f32 v33, 1.0, v33\n"
    "v_add_f32 v34, 1.0, v34\n"
    "v_add_f32 v35, 1.0, v35\n"
    "v_rcp_f32 v32, v32\n"
    "s_cbranch_vccnz .Lfz_RET_bX22\n"
    "s_barrier\n"
    ".Lfz_RET_bX22:\n"
    "s_waitcnt lgkmcnt(4)\n"
    "v_mfma_f32_16x16x32_bf16 v[96:99], v[16:19], v[160:163], v[96:99]\n"
    "ds_read_b64_tr_b16 v[28:29], v2 offset:8192\n"
    "v_mfma_f32_16x16x32_bf16 v[100:103], v[16:19], v[164:167], v[100:103]\n"
    "ds_read_b64_tr_b16 v[30:31], v2 offset:8704\n"
    "s_waitcnt lgkmcnt(4)\n"
    "v_mfma_f32_16x16x32_bf16 v[108:111], v[20:23], v[164:167], v[108:111]\n"
    "ds_read_b64_tr_b16 v[16:17], v1 offset:4096\n"
    "v_mfma_f32_16x16x32_bf16 v[104:107], v[20:23], v[160:163], v[104:107]\n"
    "ds_read_b64_tr_b16 v[18:19], v1 offset:4608\n"
    "s_waitcnt lgkmcnt(4)\n"
    "v_mfma_f32_16x16x32_bf16 v[112:115], v[24:27], v[160:163], v[112:115]\n"
    "ds_read_b64_tr_b16 v[20:21], v2 offset:4096\n"
    "v_mfma_f32_16x16x32_bf16 v[116:119], v[24:27], v[164:167], v[116:119]\n"
    "ds_read_b64_tr_b16 v[22:23], v2 offset:4608\n"
    "s_waitcnt lgkmcnt(4)\n"
    "v_mfma_f32_16x16x32_bf16 v[124:127], v[28:31], v[164:167], v[124:127]\n"
    "ds_read_b64_tr_b16 v[24:25], v1 offset:12288\n"
    "v_mfma_f32_16x16x32_bf16 v[120:123], v[28:31], v[160:163], v[120:123]\n"
    "ds_read_b64_tr_b16 v[26:27], v1 offset:12800\n"
    "s_waitcnt lgkmcnt(4)\n"
    "v_mfma_f32_16x16x32_bf16 v[96:99], v[16:19], v[168:171], v[96:99]\n"
    "ds_read_b64_tr_b16 v[28:29], v2 offset:12288\n"
    "v_mfma_f32_16x16x32_bf16 v[100:103], v[16:19], v[172:175], v[100:103]\n"
    "ds_read_b64_tr_b16 v[30:31], v2 offset:12800\n"
    "s_waitcnt lgkmcnt(4)\n"
    "v_mfma_f32_16x16x32_bf16 v[108:111], v[20:23], v[172:175], v[108:111]\n"
    "ds_read_b64_tr_b16 v[16:17], v1 offset:49152\n"
    "v_mfma_f32_16x16x32_bf16 v[104:107], v[20:23], v[168:171], v[104:107]\n"
    "ds_read_b64_tr_b16 v[18:19], v1 offset:49664\n"
    "s_waitcnt lgkmcnt(4)\n"
    "v_mfma_f32_16x16x32_bf16 v[112:115], v[24:27], v[168:171], v[112:115]\n"
    "ds_read_b64_tr_b16 v[20:21], v2 offset:49152\n"
    "v_mfma_f32_16x16x32_bf16 v[116:119], v[24:27], v[172:175], v[116:119]\n"
    "ds_read_b64_tr_b16 v[22:23], v2 offset:49664\n"
    "s_waitcnt lgkmcnt(4)\n"
    "v_mfma_f32_16x16x32_bf16 v[124:127], v[28:31], v[172:175], v[124:127]\n"
    "ds_read_b64_tr_b16 v[24:25], v1 offset:57344\n"
    "v_mfma_f32_16x16x32_bf16 v[120:123], v[28:31], v[168:171], v[120:123]\n"
    "ds_read_b64_tr_b16 v[26:27], v1 offset:57856\n"
    "s_waitcnt vmcnt(12)\n"
    "s_cbranch_vccz .Lfz_RET_bX23\n"
    "s_barrier\n"
    ".Lfz_RET_bX23:\n"
    "s_add_u32 m0, s52, 0x24000\n"
    "s_mul_i32 s54, s62, 0x30000\n"
    "buffer_load_dwordx4 v7, s[16:19], s54 offen lds\n"
    "v_rcp_f32 v33, v33\n"
    "v_rcp_f32 v34, v34\n"
    "v_rcp_f32 v35, v35\n"
    "v_mul_f32 v32, v32, v88\n"
    "v_mul_f32 v33, v33, v89\n"
    "v_mul_f32 v34, v34, v90\n"
    "v_mul_f32 v35, v35, v91\n"
    "v_cvt_pk_bf16_f32 v38, v32, v33\n"
    "v_cvt_pk_bf16_f32 v39, v34, v35\n"
    "buffer_store_dwordx4 v[36:39], v10, s[44:47], s67 offen offset:0 sc1\n"
    "s_add_u32 m0, s52, 0x26000\n"
    "s_mul_i32 s55, s63, 0x30000\n"
    "buffer_load_dwordx4 v7, s[16:19], s55 offen lds\n"
    "v_mul_f32 v32, v68, v68\n"
    "v_mul_f32 v33, v69, v69\n"
    "v_mul_f32 v34, v70, v70\n"
    "v_mul_f32 v35, v71, v71\n"
    "v_fmaak_f32 v32, v40, v32, 0xc0135761\n"
    "v_fmaak_f32 v33, v40, v33, 0xc0135761\n"
    "v_fmaak_f32 v34, v40, v34, 0xc0135761\n"
    "v_fmaak_f32 v35, v40, v35, 0xc0135761\n"
    "v_mul_f32 v32, v32, v68\n"
    "v_mul_f32 v33, v33, v69\n"
    "v_mul_f32 v34, v34, v70\n"
    "v_mul_f32 v35, v35, v71\n"
    "s_cbranch_vccnz .Lfz_RET_bX24\n"
    "s_barrier\n"
    ".Lfz_RET_bX24:\n"
    "s_waitcnt lgkmcnt(4)\n"
    "v_mfma_f32_16x16x32_bf16 v[96:99], v[16:19], v[176:179], v[96:99]\n"
    "ds_read_b64_tr_b16 v[28:29], v2 offset:57344\n"
    "v_mfma_f32_16x16x32_bf16 v[100:103], v[16:19], v[180:183], v[100:103]\n"
    "ds_read_b64_tr_b16 v[30:31], v2 offset:57856\n"
    "s_waitcnt lgkmcnt(4)\n"
    "v_mfma_f32_16x16x32_bf16 v[108:111], v[20:23], v[180:183], v[108:111]\n"
    "ds_read_b64_tr_b16 v[16:17], v1 offset:53248\n"
    "v_mfma_f32_16x16x32_bf16 v[104:107], v[20:23], v[176:179], v[104:107]\n"
    "ds_read_b64_tr_b16 v[18:19], v1 offset:53760\n"
    "s_waitcnt lgkmcnt(4)\n"
    "v_mfma_f32_16x16x32_bf16 v[112:115], v[24:27], v[176:179], v[112:115]\n"
    "ds_read_b64_tr_b16 v[20:21], v2 offset:53248\n"
    "v_mfma_f32_16x16x32_bf16 v[116:119], v[24:27], v[180:183], v[116:119]\n"
    "ds_read_b64_tr_b16 v[22:23], v2 offset:53760\n"
    "s_waitcnt lgkmcnt(4)\n"
    "v_mfma_f32_16x16x32_bf16 v[124:127], v[28:31], v[180:183], v[124:127]\n"
    "ds_read_b64_tr_b16 v[24:25], v1 offset:61440\n"
    "v_mfma_f32_16x16x32_bf16 v[120:123], v[28:31], v[176:179], v[120:123]\n"
    "ds_read_b64_tr_b16 v[26:27], v1 offset:61952\n"
    "s_waitcnt lgkmcnt(4)\n"
    "v_mfma_f32_16x16x32_bf16 v[96:99], v[16:19], v[184:187], v[96:99]\n"
    "ds_read_b64_tr_b16 v[28:29], v2 offset:61440\n"
    "v_mfma_f32_16x16x32_bf16 v[100:103], v[16:19], v[188:191], v[100:103]\n"
    "ds_read_b64_tr_b16 v[30:31], v2 offset:61952\n"
    "s_waitcnt lgkmcnt(4)\n"
    "v_mfma_f32_16x16x32_bf16 v[108:111], v[20:23], v[188:191], v[108:111]\n"
    "v_add_u32 v1, 0x10000, v1\n"
    "ds_read_b64_tr_b16 v[16:17], v1 offset:0\n"
    "v_mfma_f32_16x16x32_bf16 v[104:107], v[20:23], v[184:187], v[104:107]\n"
    "ds_read_b64_tr_b16 v[18:19], v1 offset:512\n"
    "s_waitcnt lgkmcnt(4)\n"
    "v_mfma_f32_16x16x32_bf16 v[112:115], v[24:27], v[184:187], v[112:115]\n"
    "v_add_u32 v2, 0x10000, v2\n"
    "ds_read_b64_tr_b16 v[20:21], v2 offset:0\n"
    "v_mfma_f32_16x16x32_bf16 v[116:119], v[24:27], v[188:191], v[116:119]\n"
    "ds_read_b64_tr_b16 v[22:23], v2 offset:512\n"
    "s_waitcnt lgkmcnt(4)\n"
    "v_mfma_f32_16x16x32_bf16 v[124:127], v[28:31], v[188:191], v[124:127]\n"
    "ds_read_b64_tr_b16 v[24:25], v1 offset:8192\n"
    "v_mfma_f32_16x16x32_bf16 v[120:123], v[28:31], v[184:187], v[120:123]\n"
    "ds_read_b64_tr_b16 v[26:27], v1 offset:8704\n"
    "s_waitcnt vmcnt(13)\n"
    "s_cbranch_vccz .Lfz_RET_bX25\n"
    "s_barrier\n"
    ".Lfz_RET_bX25:\n"
    "s_add_u32 m0, s52, 0x0\n"
    "s_mov_b32 s54, 0x0\n"
    "buffer_load_dwordx4 v9, s[36:39], s54 offen lds\n"
    "v_exp_f32 v32, v32\n"
    "v_mul_f32 v84, v84, v68\n"
    "v_exp_f32 v33, v33\n"
    "v_mul_f32 v85, v85, v69\n"
    "v_exp_f32 v34, v34\n"
    "v_mul_f32 v86, v86, v70\n"
    "v_exp_f32 v35, v35\n"
    "v_mul_f32 v87, v87, v71\n"
    "s_add_u32 m0, s52, 0x2000\n"
    "s_mov_b32 s55, 0x10000\n"
    "buffer_load_dwordx4 v9, s[36:39], s55 offen lds\n"
    "v_add_f32 v32, 1.0, v32\n"
    "v_add_f32 v33, 1.0, v33\n"
    "v_add_f32 v34, 1.0, v34\n"
    "v_add_f32 v35, 1.0, v35\n"
    "v_rcp_f32 v32, v32\n"
    "v_rcp_f32 v33, v33\n"
    "v_rcp_f32 v34, v34\n"
    "v_rcp_f32 v35, v35\n"
    "s_cbranch_vccnz .Lfz_RET_bX26\n"
    "s_barrier\n"
    ".Lfz_RET_bX26:\n"
    "s_waitcnt lgkmcnt(4)\n"
    "v_mfma_f32_16x16x32_bf16 v[96:99], v[16:19], v[192:195], v[96:99]\n"
    "ds_read_b64_tr_b16 v[28:29], v2 offset:8192\n"
    "v_mfma_f32_16x16x32_bf16 v[100:103], v[16:19], v[196:199], v[100:103]\n"
    "ds_read_b64_tr_b16 v[30:31], v2 offset:8704\n"
    "s_waitcnt lgkmcnt(4)\n"
    "v_mfma_f32_16x16x32_bf16 v[108:111], v[20:23], v[196:199], v[108:111]\n"
    "ds_read_b64_tr_b16 v[16:17], v1 offset:4096\n"
    "v_mfma_f32_16x16x32_bf16 v[104:107], v[20:23], v[192:195], v[104:107]\n"
    "ds_read_b64_tr_b16 v[18:19], v1 offset:4608\n"
    "s_waitcnt lgkmcnt(4)\n"
    "v_mfma_f32_16x16x32_bf16 v[112:115], v[24:27], v[192:195], v[112:115]\n"
    "ds_read_b64_tr_b16 v[20:21], v2 offset:4096\n"
    "v_mfma_f32_16x16x32_bf16 v[116:119], v[24:27], v[196:199], v[116:119]\n"
    "ds_read_b64_tr_b16 v[22:23], v2 offset:4608\n"
    "s_waitcnt lgkmcnt(4)\n"
    "v_mfma_f32_16x16x32_bf16 v[124:127], v[28:31], v[196:199], v[124:127]\n"
    "ds_read_b64_tr_b16 v[24:25], v1 offset:12288\n"
    "v_mfma_f32_16x16x32_bf16 v[120:123], v[28:31], v[192:195], v[120:123]\n"
    "ds_read_b64_tr_b16 v[26:27], v1 offset:12800\n"
    "s_waitcnt lgkmcnt(4)\n"
    "v_mfma_f32_16x16x32_bf16 v[96:99], v[16:19], v[200:203], v[96:99]\n"
    "ds_read_b64_tr_b16 v[28:29], v2 offset:12288\n"
    "v_mfma_f32_16x16x32_bf16 v[100:103], v[16:19], v[204:207], v[100:103]\n"
    "ds_read_b64_tr_b16 v[30:31], v2 offset:12800\n"
    "s_waitcnt lgkmcnt(4)\n"
    "v_mfma_f32_16x16x32_bf16 v[108:111], v[20:23], v[204:207], v[108:111]\n"
    "ds_read_b64_tr_b16 v[16:17], v1 offset:16384\n"
    "v_mfma_f32_16x16x32_bf16 v[104:107], v[20:23], v[200:203], v[104:107]\n"
    "ds_read_b64_tr_b16 v[18:19], v1 offset:16896\n"
    "s_waitcnt lgkmcnt(4)\n"
    "v_mfma_f32_16x16x32_bf16 v[112:115], v[24:27], v[200:203], v[112:115]\n"
    "ds_read_b64_tr_b16 v[20:21], v2 offset:16384\n"
    "v_mfma_f32_16x16x32_bf16 v[116:119], v[24:27], v[204:207], v[116:119]\n"
    "ds_read_b64_tr_b16 v[22:23], v2 offset:16896\n"
    "s_waitcnt lgkmcnt(4)\n"
    "v_mfma_f32_16x16x32_bf16 v[124:127], v[28:31], v[204:207], v[124:127]\n"
    "ds_read_b64_tr_b16 v[24:25], v1 offset:24576\n"
    "v_mfma_f32_16x16x32_bf16 v[120:123], v[28:31], v[200:203], v[120:123]\n"
    "ds_read_b64_tr_b16 v[26:27], v1 offset:25088\n"
    "s_waitcnt vmcnt(13)\n"
    "s_cbranch_vccz .Lfz_RET_bX27\n"
    "s_barrier\n"
    ".Lfz_RET_bX27:\n"
    "s_add_u32 m0, s52, 0xc000\n"
    "s_mov_b32 s54, 0x20000\n"
    "buffer_load_dwordx4 v9, s[36:39], s54 offen lds\n"
    "v_mul_f32 v32, v32, v84\n"
    "v_mul_f32 v33, v33, v85\n"
    "v_mul_f32 v34, v34, v86\n"
    "v_mul_f32 v35, v35, v87\n"
    "v_cvt_pk_bf16_f32 v36, v32, v33\n"
    "v_cvt_pk_bf16_f32 v37, v34, v35\n"
    "v_mul_f32 v32, v76, v76\n"
    "v_mul_f32 v33, v77, v77\n"
    "v_mul_f32 v34, v78, v78\n"
    "v_mul_f32 v35, v79, v79\n"
    "v_fmaak_f32 v32, v40, v32, 0xc0135761\n"
    "v_fmaak_f32 v33, v40, v33, 0xc0135761\n"
    "s_add_u32 m0, s52, 0xe000\n"
    "s_mov_b32 s55, 0x30000\n"
    "buffer_load_dwordx4 v9, s[36:39], s55 offen lds\n"
    "v_fmaak_f32 v34, v40, v34, 0xc0135761\n"
    "v_fmaak_f32 v35, v40, v35, 0xc0135761\n"
    "v_mul_f32 v32, v32, v76\n"
    "v_mul_f32 v33, v33, v77\n"
    "v_mul_f32 v34, v34, v78\n"
    "v_mul_f32 v35, v35, v79\n"
    "v_exp_f32 v32, v32\n"
    "v_mul_f32 v92, v92, v76\n"
    "v_exp_f32 v33, v33\n"
    "v_mul_f32 v93, v93, v77\n"
    "s_cbranch_vccnz .Lfz_RET_bX28\n"
    "s_barrier\n"
    ".Lfz_RET_bX28:\n"
    "s_waitcnt lgkmcnt(4)\n"
    "v_mfma_f32_16x16x32_bf16 v[96:99], v[16:19], v[208:211], v[96:99]\n"
    "ds_read_b64_tr_b16 v[28:29], v2 offset:24576\n"
    "v_mfma_f32_16x16x32_bf16 v[100:103], v[16:19], v[212:215], v[100:103]\n"
    "ds_read_b64_tr_b16 v[30:31], v2 offset:25088\n"
    "s_waitcnt lgkmcnt(4)\n"
    "v_mfma_f32_16x16x32_bf16 v[108:111], v[20:23], v[212:215], v[108:111]\n"
    "ds_read_b64_tr_b16 v[16:17], v1 offset:20480\n"
    "v_mfma_f32_16x16x32_bf16 v[104:107], v[20:23], v[208:211], v[104:107]\n"
    "ds_read_b64_tr_b16 v[18:19], v1 offset:20992\n"
    "s_waitcnt lgkmcnt(4)\n"
    "v_mfma_f32_16x16x32_bf16 v[112:115], v[24:27], v[208:211], v[112:115]\n"
    "ds_read_b64_tr_b16 v[20:21], v2 offset:20480\n"
    "v_mfma_f32_16x16x32_bf16 v[116:119], v[24:27], v[212:215], v[116:119]\n"
    "ds_read_b64_tr_b16 v[22:23], v2 offset:20992\n"
    "s_waitcnt lgkmcnt(4)\n"
    "v_mfma_f32_16x16x32_bf16 v[124:127], v[28:31], v[212:215], v[124:127]\n"
    "ds_read_b64_tr_b16 v[24:25], v1 offset:28672\n"
    "v_mfma_f32_16x16x32_bf16 v[120:123], v[28:31], v[208:211], v[120:123]\n"
    "ds_read_b64_tr_b16 v[26:27], v1 offset:29184\n"
    "s_waitcnt lgkmcnt(4)\n"
    "v_mfma_f32_16x16x32_bf16 v[96:99], v[16:19], v[216:219], v[96:99]\n"
    "ds_read_b64_tr_b16 v[28:29], v2 offset:28672\n"
    "v_mfma_f32_16x16x32_bf16 v[100:103], v[16:19], v[220:223], v[100:103]\n"
    "ds_read_b64_tr_b16 v[30:31], v2 offset:29184\n"
    "s_waitcnt lgkmcnt(4)\n"
    "v_mfma_f32_16x16x32_bf16 v[108:111], v[20:23], v[220:223], v[108:111]\n"
    "ds_read_b64_tr_b16 v[16:17], v1 offset:32768\n"
    "v_mfma_f32_16x16x32_bf16 v[104:107], v[20:23], v[216:219], v[104:107]\n"
    "ds_read_b64_tr_b16 v[18:19], v1 offset:33280\n"
    "s_waitcnt lgkmcnt(4)\n"
    "v_mfma_f32_16x16x32_bf16 v[112:115], v[24:27], v[216:219], v[112:115]\n"
    "ds_read_b64_tr_b16 v[20:21], v2 offset:32768\n"
    "v_mfma_f32_16x16x32_bf16 v[116:119], v[24:27], v[220:223], v[116:119]\n"
    "ds_read_b64_tr_b16 v[22:23], v2 offset:33280\n"
    "s_waitcnt lgkmcnt(4)\n"
    "v_mfma_f32_16x16x32_bf16 v[124:127], v[28:31], v[220:223], v[124:127]\n"
    "ds_read_b64_tr_b16 v[24:25], v1 offset:40960\n"
    "v_mfma_f32_16x16x32_bf16 v[120:123], v[28:31], v[216:219], v[120:123]\n"
    "ds_read_b64_tr_b16 v[26:27], v1 offset:41472\n"
    "s_waitcnt vmcnt(13)\n"
    "s_cbranch_vccz .Lfz_RET_bX29\n"
    "s_barrier\n"
    ".Lfz_RET_bX29:\n"
    "s_add_u32 m0, s52, 0x10000\n"
    "s_mov_b32 s54, 0x40000\n"
    "buffer_load_dwordx4 v9, s[36:39], s54 offen lds\n"
    "v_exp_f32 v34, v34\n"
    "v_mul_f32 v94, v94, v78\n"
    "v_exp_f32 v35, v35\n"
    "v_mul_f32 v95, v95, v79\n"
    "v_add_f32 v32, 1.0, v32\n"
    "v_add_f32 v33, 1.0, v33\n"
    "v_add_f32 v34, 1.0, v34\n"
    "v_add_f32 v35, 1.0, v35\n"
    "v_rcp_f32 v32, v32\n"
    "s_add_u32 m0, s52, 0x12000\n"
    "s_mov_b32 s55, 0x50000\n"
    "buffer_load_dwordx4 v9, s[36:39], s55 offen lds\n"
    "v_rcp_f32 v33, v33\n"
    "v_rcp_f32 v34, v34\n"
    "v_rcp_f32 v35, v35\n"
    "v_mul_f32 v32, v32, v92\n"
    "v_mul_f32 v33, v33, v93\n"
    "v_mul_f32 v34, v34, v94\n"
    "v_mul_f32 v35, v35, v95\n"
    "v_cvt_pk_bf16_f32 v38, v32, v33\n"
    "v_cvt_pk_bf16_f32 v39, v34, v35\n"
    "s_cbranch_vccnz .Lfz_RET_bX30\n"
    "s_barrier\n"
    ".Lfz_RET_bX30:\n"
    "s_waitcnt lgkmcnt(4)\n"
    "v_mfma_f32_16x16x32_bf16 v[96:99], v[16:19], v[224:227], v[96:99]\n"
    "ds_read_b64_tr_b16 v[28:29], v2 offset:40960\n"
    "v_mfma_f32_16x16x32_bf16 v[100:103], v[16:19], v[228:231], v[100:103]\n"
    "ds_read_b64_tr_b16 v[30:31], v2 offset:41472\n"
    "s_waitcnt lgkmcnt(4)\n"
    "v_mfma_f32_16x16x32_bf16 v[108:111], v[20:23], v[228:231], v[108:111]\n"
    "ds_read_b64_tr_b16 v[16:17], v1 offset:36864\n"
    "v_mfma_f32_16x16x32_bf16 v[104:107], v[20:23], v[224:227], v[104:107]\n"
    "ds_read_b64_tr_b16 v[18:19], v1 offset:37376\n"
    "s_waitcnt lgkmcnt(4)\n"
    "v_mfma_f32_16x16x32_bf16 v[112:115], v[24:27], v[224:227], v[112:115]\n"
    "ds_read_b64_tr_b16 v[20:21], v2 offset:36864\n"
    "v_mfma_f32_16x16x32_bf16 v[116:119], v[24:27], v[228:231], v[116:119]\n"
    "ds_read_b64_tr_b16 v[22:23], v2 offset:37376\n"
    "s_waitcnt lgkmcnt(4)\n"
    "v_mfma_f32_16x16x32_bf16 v[124:127], v[28:31], v[228:231], v[124:127]\n"
    "ds_read_b64_tr_b16 v[24:25], v1 offset:45056\n"
    "v_mfma_f32_16x16x32_bf16 v[120:123], v[28:31], v[224:227], v[120:123]\n"
    "ds_read_b64_tr_b16 v[26:27], v1 offset:45568\n"
    "s_waitcnt lgkmcnt(4)\n"
    "v_mfma_f32_16x16x32_bf16 v[96:99], v[16:19], v[232:235], v[96:99]\n"
    "ds_read_b64_tr_b16 v[28:29], v2 offset:45056\n"
    "v_mfma_f32_16x16x32_bf16 v[100:103], v[16:19], v[236:239], v[100:103]\n"
    "ds_read_b64_tr_b16 v[30:31], v2 offset:45568\n"
    "s_waitcnt lgkmcnt(4)\n"
    "v_mfma_f32_16x16x32_bf16 v[108:111], v[20:23], v[236:239], v[108:111]\n"
    "v_add_u32 v1, 0xffff4000, v1\n"
    "ds_read_b64_tr_b16 v[16:17], v1 offset:0\n"
    "v_mfma_f32_16x16x32_bf16 v[104:107], v[20:23], v[232:235], v[104:107]\n"
    "ds_read_b64_tr_b16 v[18:19], v1 offset:512\n"
    "s_waitcnt lgkmcnt(4)\n"
    "v_mfma_f32_16x16x32_bf16 v[112:115], v[24:27], v[232:235], v[112:115]\n"
    "v_add_u32 v2, 0xffff4000, v2\n"
    "ds_read_b64_tr_b16 v[20:21], v2 offset:0\n"
    "v_mfma_f32_16x16x32_bf16 v[116:119], v[24:27], v[236:239], v[116:119]\n"
    "ds_read_b64_tr_b16 v[22:23], v2 offset:512\n"
    "s_waitcnt lgkmcnt(4)\n"
    "v_mfma_f32_16x16x32_bf16 v[124:127], v[28:31], v[236:239], v[124:127]\n"
    "ds_read_b64_tr_b16 v[24:25], v1 offset:8192\n"
    "v_mfma_f32_16x16x32_bf16 v[120:123], v[28:31], v[232:235], v[120:123]\n"
    "ds_read_b64_tr_b16 v[26:27], v1 offset:8704\n"
    "s_waitcnt vmcnt(13)\n"
    "s_cbranch_vccz .Lfz_RET_bX31\n"
    "s_barrier\n"
    ".Lfz_RET_bX31:\n"
    "s_add_u32 m0, s52, 0x14000\n"
    "s_mov_b32 s54, 0x60000\n"
    "buffer_load_dwordx4 v9, s[36:39], s54 offen lds\n"
    "buffer_store_dwordx4 v[36:39], v10, s[44:47], s67 offen offset:1024 sc1\n"
    "s_add_u32 m0, s52, 0x16000\n"
    "s_mov_b32 s55, 0x70000\n"
    "buffer_load_dwordx4 v9, s[36:39], s55 offen lds\n"
    "s_cbranch_vccnz .Lfz_RET_bX32\n"
    "s_barrier\n"
    ".Lfz_RET_bX32:\n"
    "s_waitcnt lgkmcnt(4)\n"
    "v_mfma_f32_16x16x32_bf16 v[96:99], v[16:19], v[240:243], v[96:99]\n"
    "ds_read_b64_tr_b16 v[28:29], v2 offset:8192\n"
    "v_mfma_f32_16x16x32_bf16 v[100:103], v[16:19], v[244:247], v[100:103]\n"
    "ds_read_b64_tr_b16 v[30:31], v2 offset:8704\n"
    "s_waitcnt lgkmcnt(4)\n"
    "v_mfma_f32_16x16x32_bf16 v[108:111], v[20:23], v[244:247], v[108:111]\n"
    "ds_read_b64_tr_b16 v[16:17], v1 offset:4096\n"
    "v_mfma_f32_16x16x32_bf16 v[104:107], v[20:23], v[240:243], v[104:107]\n"
    "ds_read_b64_tr_b16 v[18:19], v1 offset:4608\n"
    "s_waitcnt lgkmcnt(4)\n"
    "v_mfma_f32_16x16x32_bf16 v[112:115], v[24:27], v[240:243], v[112:115]\n"
    "ds_read_b64_tr_b16 v[20:21], v2 offset:4096\n"
    "v_mfma_f32_16x16x32_bf16 v[116:119], v[24:27], v[244:247], v[116:119]\n"
    "ds_read_b64_tr_b16 v[22:23], v2 offset:4608\n"
    "s_waitcnt lgkmcnt(4)\n"
    "v_mfma_f32_16x16x32_bf16 v[124:127], v[28:31], v[244:247], v[124:127]\n"
    "ds_read_b64_tr_b16 v[24:25], v1 offset:12288\n"
    "v_mfma_f32_16x16x32_bf16 v[120:123], v[28:31], v[240:243], v[120:123]\n"
    "ds_read_b64_tr_b16 v[26:27], v1 offset:12800\n"
    "s_waitcnt lgkmcnt(4)\n"
    "v_mfma_f32_16x16x32_bf16 v[96:99], v[16:19], v[248:251], v[96:99]\n"
    "ds_read_b64_tr_b16 v[28:29], v2 offset:12288\n"
    "v_mfma_f32_16x16x32_bf16 v[100:103], v[16:19], v[252:255], v[100:103]\n"
    "ds_read_b64_tr_b16 v[30:31], v2 offset:12800\n"
    "s_waitcnt lgkmcnt(4)\n"
    "v_mfma_f32_16x16x32_bf16 v[108:111], v[20:23], v[252:255], v[108:111]\n"
    "ds_read_b64_tr_b16 v[16:17], v1 offset:16384\n"
    "v_mfma_f32_16x16x32_bf16 v[104:107], v[20:23], v[248:251], v[104:107]\n"
    "ds_read_b64_tr_b16 v[18:19], v1 offset:16896\n"
    "s_waitcnt lgkmcnt(4)\n"
    "v_mfma_f32_16x16x32_bf16 v[112:115], v[24:27], v[248:251], v[112:115]\n"
    "ds_read_b64_tr_b16 v[20:21], v2 offset:16384\n"
    "v_mfma_f32_16x16x32_bf16 v[116:119], v[24:27], v[252:255], v[116:119]\n"
    "ds_read_b64_tr_b16 v[22:23], v2 offset:16896\n"
    "s_waitcnt lgkmcnt(4)\n"
    "v_mfma_f32_16x16x32_bf16 v[124:127], v[28:31], v[252:255], v[124:127]\n"
    "ds_read_b64_tr_b16 v[24:25], v1 offset:20480\n"
    "v_mfma_f32_16x16x32_bf16 v[120:123], v[28:31], v[248:251], v[120:123]\n"
    "ds_read_b64_tr_b16 v[26:27], v1 offset:20992\n"
    "s_waitcnt vmcnt(14)\n"
    "s_cbranch_vccz .Lfz_RET_bX33\n"
    "s_barrier\n"
    ".Lfz_RET_bX33:\n"
    "s_add_u32 m0, s52, 0x18000\n"
    "s_mov_b32 s54, 0x180000\n"
    "buffer_load_dwordx4 v9, s[36:39], s54 offen lds\n"
    "s_waitcnt vmcnt(2)\n"
    "s_mov_b64 exec, 1\n"
    "buffer_atomic_add v12, off, s[44:47], s64 offset:0\n"
    "s_mov_b64 exec, -1\n"
    "v_mov_b32 v92, 0xbdd2d3e8\n"
    "v_mov_b32 v94, 0xc0135761\n"
    "v_mul_f32 v32, v96, v96\n"
    "v_mul_f32 v33, v97, v97\n"
    "v_mul_f32 v34, v98, v98\n"
    "v_mul_f32 v35, v99, v99\n"
    "v_fmaak_f32 v32, v92, v32, 0xc0135761\n"
    "v_fmaak_f32 v33, v92, v33, 0xc0135761\n"
    "v_fmaak_f32 v34, v92, v34, 0xc0135761\n"
    "v_fmaak_f32 v35, v92, v35, 0xc0135761\n"
    "v_mul_f32 v32, v32, v96\n"
    "v_mul_f32 v33, v33, v97\n"
    "v_mul_f32 v34, v34, v98\n"
    "v_mul_f32 v35, v35, v99\n"
    "v_exp_f32 v32, v32\n"
    "v_mul_f32 v112, v112, v96\n"
    "v_exp_f32 v33, v33\n"
    "v_mul_f32 v113, v113, v97\n"
    "v_exp_f32 v34, v34\n"
    "v_mul_f32 v114, v114, v98\n"
    "v_exp_f32 v35, v35\n"
    "v_mul_f32 v115, v115, v99\n"
    "v_add_f32 v32, 1.0, v32\n"
    "v_add_f32 v33, 1.0, v33\n"
    "s_add_u32 m0, s52, 0x1a000\n"
    "s_mov_b32 s55, 0x190000\n"
    "buffer_load_dwordx4 v9, s[36:39], s55 offen lds\n"
    "v_add_f32 v34, 1.0, v34\n"
    "v_add_f32 v35, 1.0, v35\n"
    "v_rcp_f32 v32, v32\n"
    "v_rcp_f32 v33, v33\n"
    "v_rcp_f32 v34, v34\n"
    "v_rcp_f32 v35, v35\n"
    "v_mul_f32 v32, v32, v112\n"
    "v_mul_f32 v33, v33, v113\n"
    "v_mul_f32 v34, v34, v114\n"
    "v_mul_f32 v35, v35, v115\n"
    "v_cvt_pk_bf16_f32 v36, v32, v33\n"
    "v_cvt_pk_bf16_f32 v37, v34, v35\n"
    "v_pk_mul_f32 v[32:33], v[104:105], v[104:105]\n"
    "v_pk_mul_f32 v[34:35], v[106:107], v[106:107]\n"
    "v_pk_mul_f32 v[120:121], v[120:121], v[104:105]\n"
    "v_pk_mul_f32 v[122:123], v[122:123], v[106:107]\n"
    "v_pk_fma_f32 v[32:33], v[32:33], v[92:93], v[94:95] op_sel_hi:[1,0,0]\n"
    "v_pk_fma_f32 v[34:35], v[34:35], v[92:93], v[94:95] op_sel_hi:[1,0,0]\n"
    "v_pk_mul_f32 v[32:33], v[32:33], v[104:105]\n"
    "v_pk_mul_f32 v[34:35], v[34:35], v[106:107]\n"
    "v_exp_f32 v32, v32\n"
    "v_exp_f32 v33, v33\n"
    "s_cbranch_vccnz .Lfz_RET_bX34\n"
    "s_barrier\n"
    ".Lfz_RET_bX34:\n"
    "s_waitcnt lgkmcnt(4)\n"
    "v_mfma_f32_16x16x32_bf16 v[64:67], v[16:19], v[128:131], 0\n"
    "ds_read_b64_tr_b16 v[28:29], v2 offset:20480\n"
    "v_mfma_f32_16x16x32_bf16 v[68:71], v[16:19], v[132:135], 0\n"
    "ds_read_b64_tr_b16 v[30:31], v2 offset:20992\n"
    "s_waitcnt lgkmcnt(4)\n"
    "v_mfma_f32_16x16x32_bf16 v[76:79], v[20:23], v[132:135], 0\n"
    "ds_read_b64_tr_b16 v[16:17], v1 offset:24576\n"
    "v_mfma_f32_16x16x32_bf16 v[72:75], v[20:23], v[128:131], 0\n"
    "ds_read_b64_tr_b16 v[18:19], v1 offset:25088\n"
    "s_waitcnt lgkmcnt(4)\n"
    "v_mfma_f32_16x16x32_bf16 v[64:67], v[24:27], v[136:139], v[64:67]\n"
    "ds_read_b64_tr_b16 v[20:21], v2 offset:24576\n"
    "v_mfma_f32_16x16x32_bf16 v[68:71], v[24:27], v[140:143], v[68:71]\n"
    "ds_read_b64_tr_b16 v[22:23], v2 offset:25088\n"
    "s_waitcnt lgkmcnt(4)\n"
    "v_mfma_f32_16x16x32_bf16 v[76:79], v[28:31], v[140:143], v[76:79]\n"
    "ds_read_b64_tr_b16 v[24:25], v1 offset:28672\n"
    "v_mfma_f32_16x16x32_bf16 v[72:75], v[28:31], v[136:139], v[72:75]\n"
    "ds_read_b64_tr_b16 v[26:27], v1 offset:29184\n"
    "s_waitcnt lgkmcnt(4)\n"
    "v_mfma_f32_16x16x32_bf16 v[64:67], v[16:19], v[144:147], v[64:67]\n"
    "ds_read_b64_tr_b16 v[28:29], v2 offset:28672\n"
    "v_mfma_f32_16x16x32_bf16 v[68:71], v[16:19], v[148:151], v[68:71]\n"
    "ds_read_b64_tr_b16 v[30:31], v2 offset:29184\n"
    "s_waitcnt lgkmcnt(4)\n"
    "v_mfma_f32_16x16x32_bf16 v[76:79], v[20:23], v[148:151], v[76:79]\n"
    "v_add_u32 v1, 0x18000, v1\n"
    "ds_read_b64_tr_b16 v[16:17], v1 offset:0\n"
    "v_mfma_f32_16x16x32_bf16 v[72:75], v[20:23], v[144:147], v[72:75]\n"
    "ds_read_b64_tr_b16 v[18:19], v1 offset:512\n"
    "s_waitcnt lgkmcnt(4)\n"
    "v_mfma_f32_16x16x32_bf16 v[64:67], v[24:27], v[152:155], v[64:67]\n"
    "v_add_u32 v2, 0x18000, v2\n"
    "ds_read_b64_tr_b16 v[20:21], v2 offset:0\n"
    "v_mfma_f32_16x16x32_bf16 v[68:71], v[24:27], v[156:159], v[68:71]\n"
    "ds_read_b64_tr_b16 v[22:23], v2 offset:512\n"
    "s_waitcnt lgkmcnt(4)\n"
    "v_mfma_f32_16x16x32_bf16 v[76:79], v[28:31], v[156:159], v[76:79]\n"
    "ds_read_b64_tr_b16 v[24:25], v1 offset:4096\n"
    "v_mfma_f32_16x16x32_bf16 v[72:75], v[28:31], v[152:155], v[72:75]\n"
    "ds_read_b64_tr_b16 v[26:27], v1 offset:4608\n"
    "s_cbranch_vccz .Lfz_RET_bX35\n"
    "s_barrier\n"
    ".Lfz_RET_bX35:\n"
    "s_add_u32 m0, s52, 0x4000\n"
    "s_mov_b32 s54, 0x1a0000\n"
    "buffer_load_dwordx4 v9, s[36:39], s54 offen lds\n"
    "v_mov_b32 v3, 0\n"
    "s_mov_b64 exec, 1\n"
    "buffer_atomic_or v3, off, s[44:47], s70 offset:0 sc0\n"
    "s_mov_b64 exec, -1\n"
    "v_bfe_u32 v7, v0, 3, 2\n"
    "v_xor_b32 v7, s85, v7\n"
    "v_and_b32 v8, 3, v0\n"
    "v_lshlrev_b32 v8, 3, v8\n"
    "v_lshl_or_b32 v7, v7, 5, v8\n"
    "v_lshrrev_b32 v8, 2, v0\n"
    "v_lshl_or_b32 v7, v8, 7, v7\n"
    "s_lshl_b32 s87, s85, 12\n"
    "v_add_u32 v6, s87, v7\n"
    "v_xor_b32 v7, 32, v6\n"
    "v_xor_b32 v8, 64, v6\n"
    "v_xor_b32 v4, 0x60, v6\n"
    "v_exp_f32 v34, v34\n"
    "v_exp_f32 v35, v35\n"
    "v_pk_add_f32 v[32:33], v[32:33], 1.0 op_sel_hi:[1,0]\n"
    "v_pk_add_f32 v[34:35], v[34:35], 1.0 op_sel_hi:[1,0]\n"
    "v_rcp_f32 v32, v32\n"
    "v_rcp_f32 v33, v33\n"
    "v_rcp_f32 v34, v34\n"
    "v_rcp_f32 v35, v35\n"
    "s_add_u32 m0, s52, 0x6000\n"
    "s_mov_b32 s55, 0x1b0000\n"
    "buffer_load_dwordx4 v9, s[36:39], s55 offen lds\n"
    "v_pk_mul_f32 v[32:33], v[32:33], v[120:121]\n"
    "v_pk_mul_f32 v[34:35], v[34:35], v[122:123]\n"
    "v_cvt_pk_bf16_f32 v38, v32, v33\n"
    "v_cvt_pk_bf16_f32 v39, v34, v35\n"
    "buffer_store_dwordx4 v[36:39], v10, s[44:47], s68 offen offset:0 sc1\n"
    "v_mul_f32 v32, v100, v100\n"
    "v_mul_f32 v33, v101, v101\n"
    "v_mul_f32 v34, v102, v102\n"
    "v_mul_f32 v35, v103, v103\n"
    "v_fmaak_f32 v32, v92, v32, 0xc0135761\n"
    "v_fmaak_f32 v33, v92, v33, 0xc0135761\n"
    "v_fmaak_f32 v34, v92, v34, 0xc0135761\n"
    "v_fmaak_f32 v35, v92, v35, 0xc0135761\n"
    "v_mul_f32 v32, v32, v100\n"
    "v_mul_f32 v33, v33, v101\n"
    "v_mul_f32 v34, v34, v102\n"
    "v_mul_f32 v35, v35, v103\n"
    "v_exp_f32 v32, v32\n"
    "v_mul_f32 v116, v116, v100\n"
    "v_exp_f32 v33, v33\n"
    "v_mul_f32 v117, v117, v101\n"
    "v_exp_f32 v34, v34\n"
    "v_mul_f32 v118, v118, v102\n"
    "v_exp_f32 v35, v35\n"
    "s_cbranch_vccnz .Lfz_RET_bX36\n"
    "s_barrier\n"
    ".Lfz_RET_bX36:\n"
    "s_waitcnt lgkmcnt(4)\n"
    "v_mfma_f32_16x16x32_bf16 v[64:67], v[16:19], v[160:163], v[64:67]\n"
    "ds_read_b64_tr_b16 v[28:29], v2 offset:4096\n"
    "v_mfma_f32_16x16x32_bf16 v[68:71], v[16:19], v[164:167], v[68:71]\n"
    "ds_read_b64_tr_b16 v[30:31], v2 offset:4608\n"
    "s_waitcnt lgkmcnt(4)\n"
    "v_mfma_f32_16x16x32_bf16 v[76:79], v[20:23], v[164:167], v[76:79]\n"
    "ds_read_b64_tr_b16 v[16:17], v1 offset:8192\n"
    "v_mfma_f32_16x16x32_bf16 v[72:75], v[20:23], v[160:163], v[72:75]\n"
    "ds_read_b64_tr_b16 v[18:19], v1 offset:8704\n"
    "s_waitcnt lgkmcnt(4)\n"
    "v_mfma_f32_16x16x32_bf16 v[64:67], v[24:27], v[168:171], v[64:67]\n"
    "ds_read_b64_tr_b16 v[20:21], v2 offset:8192\n"
    "v_mfma_f32_16x16x32_bf16 v[68:71], v[24:27], v[172:175], v[68:71]\n"
    "ds_read_b64_tr_b16 v[22:23], v2 offset:8704\n"
    "s_waitcnt lgkmcnt(4)\n"
    "v_mfma_f32_16x16x32_bf16 v[76:79], v[28:31], v[172:175], v[76:79]\n"
    "ds_read_b64_tr_b16 v[24:25], v1 offset:12288\n"
    "v_mfma_f32_16x16x32_bf16 v[72:75], v[28:31], v[168:171], v[72:75]\n"
    "ds_read_b64_tr_b16 v[26:27], v1 offset:12800\n"
    "s_waitcnt lgkmcnt(4)\n"
    "v_mfma_f32_16x16x32_bf16 v[64:67], v[16:19], v[176:179], v[64:67]\n"
    "ds_read_b64_tr_b16 v[28:29], v2 offset:12288\n"
    "v_mfma_f32_16x16x32_bf16 v[68:71], v[16:19], v[180:183], v[68:71]\n"
    "ds_read_b64_tr_b16 v[30:31], v2 offset:12800\n"
    "s_waitcnt lgkmcnt(4)\n"
    "v_mfma_f32_16x16x32_bf16 v[76:79], v[20:23], v[180:183], v[76:79]\n"
    "ds_read_b64_tr_b16 v[16:17], v1 offset:16384\n"
    "v_mfma_f32_16x16x32_bf16 v[72:75], v[20:23], v[176:179], v[72:75]\n"
    "ds_read_b64_tr_b16 v[18:19], v1 offset:16896\n"
    "s_waitcnt lgkmcnt(4)\n"
    "v_mfma_f32_16x16x32_bf16 v[64:67], v[24:27], v[184:187], v[64:67]\n"
    "ds_read_b64_tr_b16 v[20:21], v2 offset:16384\n"
    "v_mfma_f32_16x16x32_bf16 v[68:71], v[24:27], v[188:191], v[68:71]\n"
    "ds_read_b64_tr_b16 v[22:23], v2 offset:16896\n"
    "s_waitcnt lgkmcnt(4)\n"
    "v_mfma_f32_16x16x32_bf16 v[76:79], v[28:31], v[188:191], v[76:79]\n"
    "ds_read_b64_tr_b16 v[24:25], v1 offset:20480\n"
    "v_mfma_f32_16x16x32_bf16 v[72:75], v[28:31], v[184:187], v[72:75]\n"
    "ds_read_b64_tr_b16 v[26:27], v1 offset:20992\n"
    "s_cbranch_vccz .Lfz_RET_bX37\n"
    "s_barrier\n"
    ".Lfz_RET_bX37:\n"
    "s_add_u32 m0, s52, 0x8000\n"
    "s_mov_b32 s54, 0x80000\n"
    "buffer_load_dwordx4 v9, s[36:39], s54 offen lds\n"
    "s_waitcnt vmcnt(3)\n"
    "v_readfirstlane_b32 s90, v3\n"
    "s_sub_u32 s90, s90, s53\n"
    "s_cmp_eq_u32 s90, 16\n"
    "s_cbranch_scc0 .Lfz_SLOW_X_1\n"
    ".Lfz_RET_X_1:\n"
    "buffer_load_dwordx4 v[80:83], v41, s[44:47], s71 offen sc1\n"
    "buffer_load_dwordx4 v[84:87], v41, s[44:47], s72 offen sc1\n"
    "buffer_load_dwordx4 v[88:91], v42, s[44:47], s73 offen sc1\n"
    "v_mov_b32 v14, 0\n"
    "s_mov_b64 exec, 1\n"
    "buffer_atomic_or v14, off, s[44:47], s6 offset:0 sc0\n"
    "s_mov_b64 exec, -1\n"
    "v_mul_f32 v119, v119, v103\n"
    "v_add_f32 v32, 1.0, v32\n"
    "v_add_f32 v33, 1.0, v33\n"
    "v_add_f32 v34, 1.0, v34\n"
    "v_add_f32 v35, 1.0, v35\n"
    "v_rcp_f32 v32, v32\n"
    "v_rcp_f32 v33, v33\n"
    "v_rcp_f32 v34, v34\n"
    "v_rcp_f32 v35, v35\n"
    "v_mul_f32 v32, v32, v116\n"
    "v_mul_f32 v33, v33, v117\n"
    "v_mul_f32 v34, v34, v118\n"
    "v_mul_f32 v35, v35, v119\n"
    "v_cvt_pk_bf16_f32 v36, v32, v33\n"
    "v_cvt_pk_bf16_f32 v37, v34, v35\n"
    "v_pk_mul_f32 v[32:33], v[108:109], v[108:109]\n"
    "v_pk_mul_f32 v[34:35], v[110:111], v[110:111]\n"
    "v_pk_mul_f32 v[124:125], v[124:125], v[108:109]\n"
    "s_add_u32 m0, s52, 0xa000\n"
    "s_mov_b32 s55, 0x90000\n"
    "buffer_load_dwordx4 v9, s[36:39], s55 offen lds\n"
    "v_pk_mul_f32 v[126:127], v[126:127], v[110:111]\n"
    "v_pk_fma_f32 v[32:33], v[32:33], v[92:93], v[94:95] op_sel_hi:[1,0,0]\n"
    "v_pk_fma_f32 v[34:35], v[34:35], v[92:93], v[94:95] op_sel_hi:[1,0,0]\n"
    "v_pk_mul_f32 v[32:33], v[32:33], v[108:109]\n"
    "v_pk_mul_f32 v[34:35], v[34:35], v[110:111]\n"
    "v_exp_f32 v32, v32\n"
    "v_exp_f32 v33, v33\n"
    "v_exp_f32 v34, v34\n"
    "v_exp_f32 v35, v35\n"
    "v_pk_add_f32 v[32:33], v[32:33], 1.0 op_sel_hi:[1,0]\n"
    "v_pk_add_f32 v[34:35], v[34:35], 1.0 op_sel_hi:[1,0]\n"
    "v_rcp_f32 v32, v32\n"
    "v_rcp_f32 v33, v33\n"
    "v_rcp_f32 v34, v34\n"
    "v_rcp_f32 v35, v35\n"
    "v_pk_mul_f32 v[32:33], v[32:33], v[124:125]\n"
    "v_pk_mul_f32 v[34:35], v[34:35], v[126:127]\n"
    "v_cvt_pk_bf16_f32 v38, v32, v33\n"
    "v_cvt_pk_bf16_f32 v39, v34, v35\n"
    "buffer_store_dwordx4 v[36:39], v10, s[44:47], s68 offen offset:1024 sc1\n"
    "s_cbranch_vccnz .Lfz_RET_bX38\n"
    "s_barrier\n"
    ".Lfz_RET_bX38:\n"
    "s_waitcnt lgkmcnt(4)\n"
    "v_mfma_f32_16x16x32_bf16 v[64:67], v[16:19], v[192:195], v[64:67]\n"
    "ds_read_b64_tr_b16 v[28:29], v2 offset:20480\n"
    "v_mfma_f32_16x16x32_bf16 v[68:71], v[16:19], v[196:199], v[68:71]\n"
    "ds_read_b64_tr_b16 v[30:31], v2 offset:20992\n"
    "s_waitcnt lgkmcnt(4)\n"
    "v_mfma_f32_16x16x32_bf16 v[76:79], v[20:23], v[196:199], v[76:79]\n"
    "ds_read_b64_tr_b16 v[16:17], v1 offset:24576\n"
    "v_mfma_f32_16x16x32_bf16 v[72:75], v[20:23], v[192:195], v[72:75]\n"
    "ds_read_b64_tr_b16 v[18:19], v1 offset:25088\n"
    "s_waitcnt lgkmcnt(4)\n"
    "v_mfma_f32_16x16x32_bf16 v[64:67], v[24:27], v[200:203], v[64:67]\n"
    "ds_read_b64_tr_b16 v[20:21], v2 offset:24576\n"
    "v_mfma_f32_16x16x32_bf16 v[68:71], v[24:27], v[204:207], v[68:71]\n"
    "ds_read_b64_tr_b16 v[22:23], v2 offset:25088\n"
    "s_waitcnt lgkmcnt(4)\n"
    "v_mfma_f32_16x16x32_bf16 v[76:79], v[28:31], v[204:207], v[76:79]\n"
    "ds_read_b64_tr_b16 v[24:25], v1 offset:28672\n"
    "v_mfma_f32_16x16x32_bf16 v[72:75], v[28:31], v[200:203], v[72:75]\n"
    "ds_read_b64_tr_b16 v[26:27], v1 offset:29184\n"
    "s_waitcnt lgkmcnt(4)\n"
    "v_mfma_f32_16x16x32_bf16 v[64:67], v[16:19], v[208:211], v[64:67]\n"
    "ds_read_b64_tr_b16 v[28:29], v2 offset:28672\n"
    "v_mfma_f32_16x16x32_bf16 v[68:71], v[16:19], v[212:215], v[68:71]\n"
    "ds_read_b64_tr_b16 v[30:31], v2 offset:29184\n"
    "s_waitcnt lgkmcnt(4)\n"
    "v_mfma_f32_16x16x32_bf16 v[76:79], v[20:23], v[212:215], v[76:79]\n"
    "ds_read_b64_tr_b16 v[16:17], v1 offset:32768\n"
    "v_mfma_f32_16x16x32_bf16 v[72:75], v[20:23], v[208:211], v[72:75]\n"
    "ds_read_b64_tr_b16 v[18:19], v1 offset:33280\n"
    "s_waitcnt lgkmcnt(4)\n"
    "v_mfma_f32_16x16x32_bf16 v[64:67], v[24:27], v[216:219], v[64:67]\n"
    "ds_read_b64_tr_b16 v[20:21], v2 offset:32768\n"
    "v_mfma_f32_16x16x32_bf16 v[68:71], v[24:27], v[220:223], v[68:71]\n"
    "ds_read_b64_tr_b16 v[22:23], v2 offset:33280\n"
    "s_waitcnt lgkmcnt(4)\n"
    "v_mfma_f32_16x16x32_bf16 v[76:79], v[28:31], v[220:223], v[76:79]\n"
    "ds_read_b64_tr_b16 v[24:25], v1 offset:36864\n"
    "v_mfma_f32_16x16x32_bf16 v[72:75], v[28:31], v[216:219], v[72:75]\n"
    "ds_read_b64_tr_b16 v[26:27], v1 offset:37376\n"
    "s_cbranch_vccz .Lfz_RET_bX39\n"
    "s_barrier\n"
    ".Lfz_RET_bX39:\n"
    "s_add_u32 m0, s52, 0x1c000\n"
    "s_mov_b32 s54, 0xa0000\n"
    "buffer_load_dwordx4 v9, s[36:39], s54 offen lds\n"
    "s_waitcnt vmcnt(6)\n"
    "v_cmp_eq_u32 s[90:91], s53, v81\n"
    "v_cmp_eq_u32 s[92:93], s53, v83\n"
    "s_and_b64 s[90:91], s[90:91], s[92:93]\n"
    "s_waitcnt vmcnt(5)\n"
    "v_cmp_eq_u32 s[92:93], s53, v85\n"
    "s_and_b64 s[90:91], s[90:91], s[92:93]\n"
    "v_cmp_eq_u32 s[92:93], s53, v87\n"
    "s_and_b64 s[90:91], s[90:91], s[92:93]\n"
    "s_waitcnt vmcnt(4)\n"
    "v_cmp_eq_u32 s[92:93], s79, v89\n"
    "s_and_b64 s[90:91], s[90:91], s[92:93]\n"
    "v_cmp_eq_u32 s[92:93], s79, v91\n"
    "s_and_b64 s[90:91], s[90:91], s[92:93]\n"
    "s_cmp_eq_u64 s[90:91], -1\n"
    "s_cbranch_scc0 .Lfz_SLOW_X_2\n"
    ".Lfz_RET_X_2:\n"
    "s_add_u32 m0, s52, 0x1e000\n"
    "s_mov_b32 s55, 0xb0000\n"
    "buffer_load_dwordx4 v9, s[36:39], s55 offen lds\n"
    "v_add_f32 v80, v80, v84\n"
    "v_add_f32 v82, v82, v86\n"
    "v_add_f32_dpp v88, v88, v88 row_ror:4 row_mask:0xf bank_mask:0xf\n"
    "v_add_f32_dpp v90, v90, v90 row_ror:4 row_mask:0xf bank_mask:0xf\n"
    "v_add_f32_dpp v80, v80, v80 row_ror:8 row_mask:0xf bank_mask:0xf\n"
    "v_add_f32_dpp v82, v82, v82 row_ror:8 row_mask:0xf bank_mask:0xf\n"
    "v_add_f32_dpp v88, v88, v88 row_ror:8 row_mask:0xf bank_mask:0xf\n"
    "v_add_f32_dpp v90, v90, v90 row_ror:8 row_mask:0xf bank_mask:0xf\n"
    "v_mov_b32 v84, v80\n"
    "v_mov_b32 v85, v82\n"
    "v_mov_b32 v86, v88\n"
    "v_mov_b32 v87, v90\n"
    "v_permlane16_swap_b32 v80, v84\n"
    "v_permlane16_swap_b32 v82, v85\n"
    "v_permlane16_swap_b32 v88, v86\n"
    "v_permlane16_swap_b32 v90, v87\n"
    "v_add_f32 v80, v80, v84\n"
    "v_add_f32 v82, v82, v85\n"
    "v_add_f32 v88, v88, v86\n"
    "v_add_f32 v90, v90, v87\n"
    "v_mov_b32 v84, v80\n"
    "v_mov_b32 v85, v82\n"
    "v_mov_b32 v86, v88\n"
    "v_mov_b32 v87, v90\n"
    "v_permlane32_swap_b32 v80, v84\n"
    "v_permlane32_swap_b32 v82, v85\n"
    "v_permlane32_swap_b32 v88, v86\n"
    "v_permlane32_swap_b32 v90, v87\n"
    "v_add_f32 v80, v80, v84\n"
    "v_add_f32 v82, v82, v85\n"
    "v_add_f32 v88, v88, v86\n"
    "v_add_f32 v90, v90, v87\n"
    "v_cvt_pk_bf16_f32 v80, v80, v82\n"
    "v_cvt_pk_bf16_f32 v88, v88, v90\n"
    "v_mov_b32 v81, s53\n"
    "v_mov_b32 v89, s53\n"
    "s_mov_b64 exec, 0xff\n"
    "buffer_store_dwordx2 v[80:81], v43, s[44:47], s74 offen sc1\n"
    "buffer_store_dwordx2 v[88:89], v43, s[44:47], s75 offen offset:512 sc1\n"
    "s_mov_b64 exec, 1\n"
    "buffer_atomic_add v12, off, s[44:47], s76 offset:0\n"
    "s_mov_b64 exec, -1\n"
    "s_waitcnt vmcnt(7)\n"
    "v_readfirstlane_b32 s90, v14\n"
    "s_sub_u32 s90, s90, s53\n"
    "s_cmp_eq_u32 s90, 4\n"
    "s_cbranch_scc0 .Lfz_SLOW_X_3\n"
    ".Lfz_RET_X_3:\n"
    "s_mov_b32 s88, 0x0\n"
    "buffer_load_dwordx4 v[128:131], v11, s[44:47], s88 offen offset:0 sc1\n"
    "s_mov_b32 s88, 0x0\n"
    "buffer_load_dwordx4 v[132:135], v11, s[44:47], s88 offen offset:1024 sc1\n"
    "s_mov_b32 s88, 0x2000\n"
    "buffer_load_dwordx4 v[136:139], v11, s[44:47], s88 offen offset:0 sc1\n"
    "s_mov_b32 s88, 0x2000\n"
    "buffer_load_dwordx4 v[140:143], v11, s[44:47], s88 offen offset:1024 sc1\n"
    "s_mov_b32 s88, 0x4000\n"
    "buffer_load_dwordx4 v[144:147], v11, s[44:47], s88 offen offset:0 sc1\n"
    "s_mov_b32 s88, 0x4000\n"
    "buffer_load_dwordx4 v[148:151], v11, s[44:47], s88 offen offset:1024 sc1\n"
    "s_mov_b32 s88, 0x6000\n"
    "buffer_load_dwordx4 v[152:155], v11, s[44:47], s88 offen offset:0 sc1\n"
    "s_mov_b32 s88, 0x6000\n"
    "buffer_load_dwordx4 v[156:159], v11, s[44:47], s88 offen offset:1024 sc1\n"
    "v_mov_b32 v14, 0\n"
    "s_mov_b64 exec, 1\n"
    "buffer_atomic_or v14, off, s[44:47], s6 offset:4 sc0\n"
    "s_mov_b64 exec, -1\n"
    "s_cbranch_vccnz .Lfz_RET_bX40\n"
    "s_barrier\n"
    ".Lfz_RET_bX40:\n"
    "s_waitcnt lgkmcnt(4)\n"
    "v_mfma_f32_16x16x32_bf16 v[64:67], v[16:19], v[224:227], v[64:67]\n"
    "ds_read_b64_tr_b16 v[28:29], v2 offset:36864\n"
    "v_mfma_f32_16x16x32_bf16 v[68:71], v[16:19], v[228:231], v[68:71]\n"
    "ds_read_b64_tr_b16 v[30:31], v2 offset:37376\n"
    "s_waitcnt lgkmcnt(4)\n"
    "v_mfma_f32_16x16x32_bf16 v[76:79], v[20:23], v[228:231], v[76:79]\n"
    "ds_read_b64_tr_b16 v[16:17], v1 offset:40960\n"
    "v_mfma_f32_16x16x32_bf16 v[72:75], v[20:23], v[224:227], v[72:75]\n"
    "ds_read_b64_tr_b16 v[18:19], v1 offset:41472\n"
    "s_waitcnt lgkmcnt(4)\n"
    "v_mfma_f32_16x16x32_bf16 v[64:67], v[24:27], v[232:235], v[64:67]\n"
    "ds_read_b64_tr_b16 v[20:21], v2 offset:40960\n"
    "v_mfma_f32_16x16x32_bf16 v[68:71], v[24:27], v[236:239], v[68:71]\n"
    "ds_read_b64_tr_b16 v[22:23], v2 offset:41472\n"
    "s_waitcnt lgkmcnt(4)\n"
    "v_mfma_f32_16x16x32_bf16 v[76:79], v[28:31], v[236:239], v[76:79]\n"
    "ds_read_b64_tr_b16 v[24:25], v1 offset:45056\n"
    "v_mfma_f32_16x16x32_bf16 v[72:75], v[28:31], v[232:235], v[72:75]\n"
    "ds_read_b64_tr_b16 v[26:27], v1 offset:45568\n"
    "s_waitcnt lgkmcnt(4)\n"
    "v_mfma_f32_16x16x32_bf16 v[64:67], v[16:19], v[240:243], v[64:67]\n"
    "ds_read_b64_tr_b16 v[28:29], v2 offset:45056\n"
    "v_mfma_f32_16x16x32_bf16 v[68:71], v[16:19], v[244:247], v[68:71]\n"
    "ds_read_b64_tr_b16 v[30:31], v2 offset:45568\n"
    "s_waitcnt lgkmcnt(4)\n"
    "v_mfma_f32_16x16x32_bf16 v[76:79], v[20:23], v[244:247], v[76:79]\n"
    "ds_read_b64_tr_b16 v[16:17], v6 offset:0\n"
    "v_mfma_f32_16x16x32_bf16 v[72:75], v[20:23], v[240:243], v[72:75]\n"
    "ds_read_b64_tr_b16 v[18:19], v6 offset:2048\n"
    "s_waitcnt lgkmcnt(4)\n"
    "v_mfma_f32_16x16x32_bf16 v[64:67], v[24:27], v[248:251], v[64:67]\n"
    "ds_read_b64_tr_b16 v[20:21], v7 offset:0\n"
    "v_mfma_f32_16x16x32_bf16 v[68:71], v[24:27], v[252:255], v[68:71]\n"
    "ds_read_b64_tr_b16 v[22:23], v7 offset:2048\n"
    "s_waitcnt lgkmcnt(4)\n"
    "v_mfma_f32_16x16x32_bf16 v[76:79], v[28:31], v[252:255], v[76:79]\n"
    "ds_read_b64_tr_b16 v[24:25], v8 offset:0\n"
    "v_mfma_f32_16x16x32_bf16 v[72:75], v[28:31], v[248:251], v[72:75]\n"
    "ds_read_b64_tr_b16 v[26:27], v8 offset:2048\n"
    "s_cbranch_vccz .Lfz_RET_bX41\n"
    "s_barrier\n"
    ".Lfz_RET_bX41:\n"
    "s_add_u32 m0, s52, 0x20000\n"
    "s_mov_b32 s54, 0xc0000\n"
    "buffer_load_dwordx4 v9, s[36:39], s54 offen lds\n"
    "s_waitcnt vmcnt(15)\n"
    "s_mov_b64 exec, 1\n"
    "buffer_atomic_add v12, off, s[44:47], s64 offset:8\n"
    "s_mov_b64 exec, -1\n"
    "v_mov_b32 v3, 0\n"
    "s_mov_b64 exec, 1\n"
    "buffer_atomic_or v3, off, s[44:47], s77 offset:0 sc0\n"
    "s_mov_b64 exec, -1\n"
    "s_waitcnt vmcnt(3)\n"
    "v_readfirstlane_b32 s90, v14\n"
    "s_sub_u32 s90, s90, s53\n"
    "s_cmp_eq_u32 s90, 4\n"
    "s_cbranch_scc0 .Lfz_SLOW_X_4\n"
    ".Lfz_RET_X_4:\n"
    "v_lshlrev_b32 v5, 3, v0\n"
    "v_sub_u32 v5, v11, v5\n"
    "v_cvt_pk_bf16_f32 v32, v64, v65\n"
    "v_cvt_pk_bf16_f32 v33, v66, v67\n"
    "v_cvt_pk_bf16_f32 v34, v72, v73\n"
    "v_cvt_pk_bf16_f32 v35, v74, v75\n"
    "v_cvt_pk_bf16_f32 v36, v68, v69\n"
    "v_cvt_pk_bf16_f32 v37, v70, v71\n"
    "v_cvt_pk_bf16_f32 v38, v76, v77\n"
    "v_cvt_pk_bf16_f32 v39, v78, v79\n"
    "v_mfma_f32_16x16x32_bf16 v[64:67], v[48:51], v[32:35], 0\n"
    "v_mfma_f32_16x16x32_bf16 v[68:71], v[52:55], v[32:35], 0\n"
    "v_mfma_f32_16x16x32_bf16 v[72:75], v[56:59], v[32:35], 0\n"
    "v_mfma_f32_16x16x32_bf16 v[76:79], v[60:63], v[32:35], 0\n"
    "v_mfma_f32_16x16x32_bf16 v[80:83], v[48:51], v[36:39], 0\n"
    "v_mfma_f32_16x16x32_bf16 v[84:87], v[52:55], v[36:39], 0\n"
    "v_mfma_f32_16x16x32_bf16 v[88:91], v[56:59], v[36:39], 0\n"
    "v_mfma_f32_16x16x32_bf16 v[92:95], v[60:63], v[36:39], 0\n"
    "s_add_u32 m0, s52, 0x22000\n"
    "s_mov_b32 s55, 0xd0000\n"
    "buffer_load_dwordx4 v9, s[36:39], s55 offen lds\n"
    "v_max3_f32 v48, v64, v65, v66\n"
    "v_max3_f32 v49, v67, v68, v69\n"
    "v_max3_f32 v48, v48, v70, v71\n"
    "v_max3_f32 v49, v49, v72, v73\n"
    "v_max3_f32 v48, v48, v74, v75\n"
    "v_max3_f32 v49, v49, v76, v77\n"
    "v_max3_f32 v48, v48, v78, v79\n"
    "v_max_f32 v48, v48, v49\n"
    "v_mov_b32 v50, 0x3fb8aa3b\n"
    "v_mul_f32 v48, 0xbfb8aa3b, v48\n"
    "v_pk_fma_f32 v[64:65], v[64:65], v[50:51], v[48:49] op_sel_hi:[1,0,0]\n"
    "v_pk_fma_f32 v[66:67], v[66:67], v[50:51], v[48:49] op_sel_hi:[1,0,0]\n"
    "v_pk_fma_f32 v[68:69], v[68:69], v[50:51], v[48:49] op_sel_hi:[1,0,0]\n"
    "v_pk_fma_f32 v[70:71], v[70:71], v[50:51], v[48:49] op_sel_hi:[1,0,0]\n"
    "v_pk_fma_f32 v[72:73], v[72:73], v[50:51], v[48:49] op_sel_hi:[1,0,0]\n"
    "v_pk_fma_f32 v[74:75], v[74:75], v[50:51], v[48:49] op_sel_hi:[1,0,0]\n"
    "v_pk_fma_f32 v[76:77], v[76:77], v[50:51], v[48:49] op_sel_hi:[1,0,0]\n"
    "v_pk_fma_f32 v[78:79], v[78:79], v[50:51], v[48:49] op_sel_hi:[1,0,0]\n"
    "v_exp_f32 v64, v64\n"
    "v_exp_f32 v65, v65\n"
    "v_exp_f32 v66, v66\n"
    "v_exp_f32 v67, v67\n"
    "v_exp_f32 v68, v68\n"
    "v_exp_f32 v69, v69\n"
    "s_mov_b32 s88, 0x18000\n"
    "buffer_load_dwordx2 v[160:161], v5, s[44:47], s88 offen offset:0 sc1\n"
    "buffer_load_dwordx2 v[162:163], v5, s[44:47], s88 offen offset:512 sc1\n"
    "v_pk_add_f32 v[48:49], v[64:65], v[66:67]\n"
    "v_exp_f32 v70, v70\n"
    "v_exp_f32 v71, v71\n"
    "v_pk_add_f32 v[48:49], v[48:49], v[68:69]\n"
    "v_exp_f32 v72, v72\n"
    "v_exp_f32 v73, v73\n"
    "v_pk_add_f32 v[48:49], v[48:49], v[70:71]\n"
    "v_exp_f32 v74, v74\n"
    "v_exp_f32 v75, v75\n"
    "v_pk_add_f32 v[48:49], v[48:49], v[72:73]\n"
    "v_exp_f32 v76, v76\n"
    "v_exp_f32 v77, v77\n"
    "v_pk_add_f32 v[48:49], v[48:49], v[74:75]\n"
    "v_exp_f32 v78, v78\n"
    "v_exp_f32 v79, v79\n"
    "v_pk_add_f32 v[48:49], v[48:49], v[76:77]\n"
    "v_pk_add_f32 v[48:49], v[48:49], v[78:79]\n"
    "v_add_f32 v48, v48, v49\n"
    "v_rcp_f32 v48, v48\n"
    "s_mov_b32 s88, 0x18000\n"
    "buffer_load_dwordx2 v[164:165], v5, s[44:47], s88 offen offset:1024 sc1\n"
    "buffer_load_dwordx2 v[166:167], v5, s[44:47], s88 offen offset:1536 sc1\n"
    "v_pk_mul_f32 v[64:65], v[64:65], v[48:49] op_sel_hi:[1,0]\n"
    "v_pk_mul_f32 v[66:67], v[66:67], v[48:49] op_sel_hi:[1,0]\n"
    "v_pk_mul_f32 v[68:69], v[68:69], v[48:49] op_sel_hi:[1,0]\n"
    "v_pk_mul_f32 v[70:71], v[70:71], v[48:49] op_sel_hi:[1,0]\n"
    "v_pk_mul_f32 v[72:73], v[72:73], v[48:49] op_sel_hi:[1,0]\n"
    "v_pk_mul_f32 v[74:75], v[74:75], v[48:49] op_sel_hi:[1,0]\n"
    "v_pk_mul_f32 v[76:77], v[76:77], v[48:49] op_sel_hi:[1,0]\n"
    "v_pk_mul_f32 v[78:79], v[78:79], v[48:49] op_sel_hi:[1,0]\n"
    "v_cvt_pk_bf16_f32 v32, v64, v65\n"
    "v_cvt_pk_bf16_f32 v33, v66, v67\n"
    "v_cvt_pk_bf16_f32 v34, v68, v69\n"
    "v_cvt_pk_bf16_f32 v35, v70, v71\n"
    "v_cvt_pk_bf16_f32 v36, v72, v73\n"
    "v_cvt_pk_bf16_f32 v37, v74, v75\n"
    "v_cvt_pk_bf16_f32 v38, v76, v77\n"
    "v_cvt_pk_bf16_f32 v39, v78, v79\n"
    "v_max3_f32 v48, v80, v81, v82\n"
    "v_max3_f32 v49, v83, v84, v85\n"
    "v_max3_f32 v48, v48, v86, v87\n"
    "v_max3_f32 v49, v49, v88, v89\n"
    "v_max3_f32 v48, v48, v90, v91\n"
    "v_max3_f32 v49, v49, v92, v93\n"
    "v_max3_f32 v48, v48, v94, v95\n"
    "v_max_f32 v48, v48, v49\n"
    "v_mov_b32 v50, 0x3fb8aa3b\n"
    "v_mul_f32 v48, 0xbfb8aa3b, v48\n"
    "v_pk_fma_f32 v[80:81], v[80:81], v[50:51], v[48:49] op_sel_hi:[1,0,0]\n"
    "v_pk_fma_f32 v[82:83], v[82:83], v[50:51], v[48:49] op_sel_hi:[1,0,0]\n"
    "v_pk_fma_f32 v[84:85], v[84:85], v[50:51], v[48:49] op_sel_hi:[1,0,0]\n"
    "v_pk_fma_f32 v[86:87], v[86:87], v[50:51], v[48:49] op_sel_hi:[1,0,0]\n"
    "v_pk_fma_f32 v[88:89], v[88:89], v[50:51], v[48:49] op_sel_hi:[1,0,0]\n"
    "s_mov_b32 s88, 0x1a000\n"
    "buffer_load_dwordx2 v[168:169], v5, s[44:47], s88 offen offset:0 sc1\n"
    "buffer_load_dwordx2 v[170:171], v5, s[44:47], s88 offen offset:512 sc1\n"
    "v_pk_fma_f32 v[90:91], v[90:91], v[50:51], v[48:49] op_sel_hi:[1,0,0]\n"
    "v_pk_fma_f32 v[92:93], v[92:93], v[50:51], v[48:49] op_sel_hi:[1,0,0]\n"
    "v_pk_fma_f32 v[94:95], v[94:95], v[50:51], v[48:49] op_sel_hi:[1,0,0]\n"
    "v_exp_f32 v80, v80\n"
    "v_exp_f32 v81, v81\n"
    "v_exp_f32 v82, v82\n"
    "v_exp_f32 v83, v83\n"
    "v_exp_f32 v84, v84\n"
    "v_exp_f32 v85, v85\n"
    "v_pk_add_f32 v[48:49], v[80:81], v[82:83]\n"
    "v_exp_f32 v86, v86\n"
    "v_exp_f32 v87, v87\n"
    "v_pk_add_f32 v[48:49], v[48:49], v[84:85]\n"
    "v_exp_f32 v88, v88\n"
    "v_exp_f32 v89, v89\n"
    "v_pk_add_f32 v[48:49], v[48:49], v[86:87]\n"
    "v_exp_f32 v90, v90\n"
    "v_exp_f32 v91, v91\n"
    "v_pk_add_f32 v[48:49], v[48:49], v[88:89]\n"
    "s_mov_b32 s88, 0x1a000\n"
    "buffer_load_dwordx2 v[172:173], v5, s[44:47], s88 offen offset:1024 sc1\n"
    "buffer_load_dwordx2 v[174:175], v5, s[44:47], s88 offen offset:1536 sc1\n"
    "v_exp_f32 v92, v92\n"
    "v_exp_f32 v93, v93\n"
    "v_pk_add_f32 v[48:49], v[48:49], v[90:91]\n"
    "v_exp_f32 v94, v94\n"
    "v_exp_f32 v95, v95\n"
    "v_pk_add_f32 v[48:49], v[48:49], v[92:93]\n"
    "v_pk_add_f32 v[48:49], v[48:49], v[94:95]\n"
    "v_add_f32 v48, v48, v49\n"
    "v_rcp_f32 v48, v48\n"
    "s_nop 0\n"
    "v_pk_mul_f32 v[80:81], v[80:81], v[48:49] op_sel_hi:[1,0]\n"
    "v_pk_mul_f32 v[82:83], v[82:83], v[48:49] op_sel_hi:[1,0]\n"
    "v_pk_mul_f32 v[84:85], v[84:85], v[48:49] op_sel_hi:[1,0]\n"
    "v_pk_mul_f32 v[86:87], v[86:87], v[48:49] op_sel_hi:[1,0]\n"
    "v_pk_mul_f32 v[88:89], v[88:89], v[48:49] op_sel_hi:[1,0]\n"
    "v_pk_mul_f32 v[90:91], v[90:91], v[48:49] op_sel_hi:[1,0]\n"
    "v_pk_mul_f32 v[92:93], v[92:93], v[48:49] op_sel_hi:[1,0]\n"
    "v_pk_mul_f32 v[94:95], v[94:95], v[48:49] op_sel_hi:[1,0]\n"
    "v_cvt_pk_bf16_f32 v56, v80, v81\n"
    "v_cvt_pk_bf16_f32 v57, v82, v83\n"
    "v_cvt_pk_bf16_f32 v58, v84, v85\n"
    "v_cvt_pk_bf16_f32 v59, v86, v87\n"
    "v_cvt_pk_bf16_f32 v60, v88, v89\n"
    "v_cvt_pk_bf16_f32 v61, v90, v91\n"
    "v_cvt_pk_bf16_f32 v62, v92, v93\n"
    "v_cvt_pk_bf16_f32 v63, v94, v95\n"
    "s_cbranch_vccnz .Lfz_RET_bX42\n"
    "s_barrier\n"
    ".Lfz_RET_bX42:\n"
    "s_waitcnt lgkmcnt(4)\n"
    "v_mfma_f32_16x16x32_bf16 v[96:99], v[16:19], v[128:131], 0\n"
    "ds_read_b64_tr_b16 v[28:29], v4 offset:0\n"
    "v_mfma_f32_16x16x32_bf16 v[100:103], v[16:19], v[132:135], 0\n"
    "ds_read_b64_tr_b16 v[30:31], v4 offset:2048\n"
    "s_waitcnt lgkmcnt(4)\n"
    "v_mfma_f32_16x16x32_bf16 v[108:111], v[20:23], v[132:135], 0\n"
    "ds_read_b64_tr_b16 v[16:17], v6 offset:49152\n"
    "v_mfma_f32_16x16x32_bf16 v[104:107], v[20:23], v[128:131], 0\n"
    "ds_read_b64_tr_b16 v[18:19], v6 offset:51200\n"
    "s_waitcnt lgkmcnt(4)\n"
    "v_mfma_f32_16x16x32_bf16 v[112:115], v[24:27], v[128:131], 0\n"
    "ds_read_b64_tr_b16 v[20:21], v7 offset:49152\n"
    "v_mfma_f32_16x16x32_bf16 v[116:119], v[24:27], v[132:135], 0\n"
    "ds_read_b64_tr_b16 v[22:23], v7 offset:51200\n"
    "s_waitcnt lgkmcnt(4)\n"
    "v_mfma_f32_16x16x32_bf16 v[124:127], v[28:31], v[132:135], 0\n"
    "ds_read_b64_tr_b16 v[24:25], v8 offset:49152\n"
    "v_mfma_f32_16x16x32_bf16 v[120:123], v[28:31], v[128:131], 0\n"
    "ds_read_b64_tr_b16 v[26:27], v8 offset:51200\n"
    "s_waitcnt lgkmcnt(4)\n"
    "v_mfma_f32_16x16x32_bf16 v[96:99], v[16:19], v[136:139], v[96:99]\n"
    "ds_read_b64_tr_b16 v[28:29], v4 offset:49152\n"
    "v_mfma_f32_16x16x32_bf16 v[100:103], v[16:19], v[140:143], v[100:103]\n"
    "ds_read_b64_tr_b16 v[30:31], v4 offset:51200\n"
    "s_waitcnt lgkmcnt(4)\n"
    "v_mfma_f32_16x16x32_bf16 v[108:111], v[20:23], v[140:143], v[108:111]\n"
    "v_add_u32 v6, 0x10000, v6\n"
    "ds_read_b64_tr_b16 v[16:17], v6 offset:0\n"
    "v_mfma_f32_16x16x32_bf16 v[104:107], v[20:23], v[136:139], v[104:107]\n"
    "ds_read_b64_tr_b16 v[18:19], v6 offset:2048\n"
    "s_waitcnt lgkmcnt(4)\n"
    "v_mfma_f32_16x16x32_bf16 v[112:115], v[24:27], v[136:139], v[112:115]\n"
    "v_add_u32 v7, 0x10000, v7\n"
    "ds_read_b64_tr_b16 v[20:21], v7 offset:0\n"
    "v_mfma_f32_16x16x32_bf16 v[116:119], v[24:27], v[140:143], v[116:119]\n"
    "ds_read_b64_tr_b16 v[22:23], v7 offset:2048\n"
    "s_waitcnt lgkmcnt(4)\n"
    "v_mfma_f32_16x16x32_bf16 v[124:127], v[28:31], v[140:143], v[124:127]\n"
    "v_add_u32 v8, 0x10000, v8\n"
    "ds_read_b64_tr_b16 v[24:25], v8 offset:0\n"
    "v_mfma_f32_16x16x32_bf16 v[120:123], v[28:31], v[136:139], v[120:123]\n"
    "ds_read_b64_tr_b16 v[26:27], v8 offset:2048\n"
    "s_cbranch_vccz .Lfz_RET_bX43\n"
    "s_barrier\n"
    ".Lfz_RET_bX43:\n"
    "s_add_u32 m0, s52, 0x24000\n"
    "s_mov_b32 s54, 0xe0000\n"
    "buffer_load_dwordx4 v9, s[36:39], s54 offen lds\n"
    "s_waitcnt vmcnt(10)\n"
    "v_readfirstlane_b32 s90, v3\n"
    "s_sub_u32 s90, s90, s53\n"
    "s_cmp_eq_u32 s90, 16\n"
    "s_cbranch_scc0 .Lfz_SLOW_X_5\n"
    ".Lfz_RET_X_5:\n"
    "buffer_load_dwordx4 v[240:243], v44, s[44:47], s78 offen sc1\n"
    "s_mov_b32 exec_lo, 3\n"
    "s_mov_b32 exec_hi, 3\n"
    "buffer_load_dwordx4 v[244:247], v44, s[44:47], s78 offen offset:512 sc1\n"
    "s_mov_b64 exec, -1\n"
    "s_add_u32 m0, s52, 0x26000\n"
    "s_mov_b32 s55, 0xf0000\n"
    "buffer_load_dwordx4 v9, s[36:39], s55 offen lds\n"
    "v_mov_b32 v14, 0\n"
    "s_mov_b64 exec, 1\n"
    "buffer_atomic_or v14, off, s[44:47], s6 offset:8 sc0\n"
    "s_mov_b64 exec, -1\n"
    "s_cbranch_vccnz .Lfz_RET_bX44\n"
    "s_barrier\n"
    ".Lfz_RET_bX44:\n"
    "s_waitcnt lgkmcnt(4)\n"
    "v_mfma_f32_16x16x32_bf16 v[96:99], v[16:19], v[144:147], v[96:99]\n"
    "v_add_u32 v4, 0x10000, v4\n"
    "ds_read_b64_tr_b16 v[28:29], v4 offset:0\n"
    "v_mfma_f32_16x16x32_bf16 v[100:103], v[16:19], v[148:151], v[100:103]\n"
    "ds_read_b64_tr_b16 v[30:31], v4 offset:2048\n"
    "s_waitcnt lgkmcnt(4)\n"
    "v_mfma_f32_16x16x32_bf16 v[108:111], v[20:23], v[148:151], v[108:111]\n"
    "ds_read_b64_tr_b16 v[16:17], v6 offset:16384\n"
    "v_mfma_f32_16x16x32_bf16 v[104:107], v[20:23], v[144:147], v[104:107]\n"
    "ds_read_b64_tr_b16 v[18:19], v6 offset:18432\n"
    "s_waitcnt lgkmcnt(4)\n"
    "v_mfma_f32_16x16x32_bf16 v[112:115], v[24:27], v[144:147], v[112:115]\n"
    "ds_read_b64_tr_b16 v[20:21], v7 offset:16384\n"
    "v_mfma_f32_16x16x32_bf16 v[116:119], v[24:27], v[148:151], v[116:119]\n"
    "ds_read_b64_tr_b16 v[22:23], v7 offset:18432\n"
    "s_waitcnt lgkmcnt(4)\n"
    "v_mfma_f32_16x16x32_bf16 v[124:127], v[28:31], v[148:151], v[124:127]\n"
    "ds_read_b64_tr_b16 v[24:25], v8 offset:16384\n"
    "v_mfma_f32_16x16x32_bf16 v[120:123], v[28:31], v[144:147], v[120:123]\n"
    "ds_read_b64_tr_b16 v[26:27], v8 offset:18432\n"
    "s_waitcnt lgkmcnt(4)\n"
    "v_mfma_f32_16x16x32_bf16 v[96:99], v[16:19], v[152:155], v[96:99]\n"
    "ds_read_b64_tr_b16 v[28:29], v4 offset:16384\n"
    "v_mfma_f32_16x16x32_bf16 v[100:103], v[16:19], v[156:159], v[100:103]\n"
    "ds_read_b64_tr_b16 v[30:31], v4 offset:18432\n"
    "s_waitcnt lgkmcnt(4)\n"
    "v_mfma_f32_16x16x32_bf16 v[108:111], v[20:23], v[156:159], v[108:111]\n"
    "ds_read_b64_tr_b16 v[16:17], v6 offset:32768\n"
    "v_mfma_f32_16x16x32_bf16 v[104:107], v[20:23], v[152:155], v[104:107]\n"
    "ds_read_b64_tr_b16 v[18:19], v6 offset:34816\n"
    "s_waitcnt lgkmcnt(4)\n"
    "v_mfma_f32_16x16x32_bf16 v[112:115], v[24:27], v[152:155], v[112:115]\n"
    "ds_read_b64_tr_b16 v[20:21], v7 offset:32768\n"
    "v_mfma_f32_16x16x32_bf16 v[116:119], v[24:27], v[156:159], v[116:119]\n"
    "ds_read_b64_tr_b16 v[22:23], v7 offset:34816\n"
    "s_waitcnt lgkmcnt(4)\n"
    "v_mfma_f32_16x16x32_bf16 v[124:127], v[28:31], v[156:159], v[124:127]\n"
    "ds_read_b64_tr_b16 v[24:25], v8 offset:32768\n"
    "v_mfma_f32_16x16x32_bf16 v[120:123], v[28:31], v[152:155], v[120:123]\n"
    "ds_read_b64_tr_b16 v[26:27], v8 offset:34816\n"
    "s_cbranch_vccz .Lfz_RET_bX45\n"
    "s_barrier\n"
    ".Lfz_RET_bX45:\n"
    "s_add_u32 m0, s52, 0x0\n"
    "s_mov_b32 s54, 0x100000\n"
    "buffer_load_dwordx4 v9, s[36:39], s54 offen lds\n"
    "s_mov_b32 s87, 0xc000\n"
    "v_add_u32_sdwa v41, s87, v45 dst_sel:DWORD dst_unused:UNUSED_PAD src0_sel:DWORD src1_sel:WORD_0\n"
    "v_add_u32_sdwa v42, s87, v45 dst_sel:DWORD dst_unused:UNUSED_PAD src0_sel:DWORD src1_sel:WORD_1\n"
    "v_add_u32_sdwa v45, s87, v47 dst_sel:DWORD dst_unused:UNUSED_PAD src0_sel:DWORD src1_sel:WORD_0\n"
    "v_add_u32_sdwa v47, s87, v47 dst_sel:DWORD dst_unused:UNUSED_PAD src0_sel:DWORD src1_sel:WORD_1\n"
    "v_add_u32 v46, s87, v46\n"
    "v_mov_b32 v43, 0\n"
    "s_add_u32 m0, s52, 0x2000\n"
    "s_mov_b32 s55, 0x110000\n"
    "buffer_load_dwordx4 v9, s[36:39], s55 offen lds\n"
    "s_waitcnt vmcnt(4)\n"
    "v_cmp_eq_u32 s[90:91], s53, v245\n"
    "v_cmp_eq_u32 s[92:93], s53, v247\n"
    "s_and_b64 s[90:91], s[90:91], s[92:93]\n"
    "s_or_b32 s90, s90, -4\n"
    "s_or_b32 s91, s91, -4\n"
    "v_cmp_eq_u32 s[92:93], s53, v241\n"
    "s_and_b64 s[90:91], s[90:91], s[92:93]\n"
    "v_cmp_eq_u32 s[92:93], s53, v243\n"
    "s_and_b64 s[90:91], s[90:91], s[92:93]\n"
    "s_cmp_eq_u64 s[90:91], -1\n"
    "s_cbranch_scc0 .Lfz_SLOW_X_6\n"
    ".Lfz_RET_X_6:\n"
    "s_waitcnt vmcnt(2)\n"
    "v_readfirstlane_b32 s90, v14\n"
    "s_sub_u32 s90, s90, s53\n"
    "s_cmp_eq_u32 s90, 4\n"
    "s_cbranch_scc0 .Lfz_SLOW_X_7\n"
    ".Lfz_RET_X_7:\n"
    "s_mov_b32 s88, 0x8000\n"
    "buffer_load_dwordx4 v[176:179], v11, s[44:47], s88 offen offset:0 sc1\n"
    "s_mov_b32 s88, 0x8000\n"
    "buffer_load_dwordx4 v[180:183], v11, s[44:47], s88 offen offset:1024 sc1\n"
    "s_mov_b32 s88, 0xa000\n"
    "buffer_load_dwordx4 v[184:187], v11, s[44:47], s88 offen offset:0 sc1\n"
    "s_mov_b32 s88, 0xa000\n"
    "buffer_load_dwordx4 v[188:191], v11, s[44:47], s88 offen offset:1024 sc1\n"
    "s_mov_b32 s88, 0xc000\n"
    "buffer_load_dwordx4 v[192:195], v11, s[44:47], s88 offen offset:0 sc1\n"
    "s_mov_b32 s88, 0xc000\n"
    "buffer_load_dwordx4 v[196:199], v11, s[44:47], s88 offen offset:1024 sc1\n"
    "s_cbranch_vccnz .Lfz_RET_bX46\n"
    "s_barrier\n"
    ".Lfz_RET_bX46:\n"
    "s_waitcnt lgkmcnt(4)\n"
    "v_mfma_f32_16x16x32_bf16 v[96:99], v[16:19], v[160:163], v[96:99]\n"
    "ds_read_b64_tr_b16 v[28:29], v4 offset:32768\n"
    "v_mfma_f32_16x16x32_bf16 v[100:103], v[16:19], v[164:167], v[100:103]\n"
    "ds_read_b64_tr_b16 v[30:31], v4 offset:34816\n"
    "ds_write2_b32 v41, v240, v242 offset0:0 offset1:2\n"
    "s_waitcnt lgkmcnt(5)\n"
    "v_mfma_f32_16x16x32_bf16 v[108:111], v[20:23], v[164:167], v[108:111]\n"
    "v_add_u32 v6, 0xffff4000, v6\n"
    "ds_read_b64_tr_b16 v[16:17], v6 offset:0\n"
    "v_mfma_f32_16x16x32_bf16 v[104:107], v[20:23], v[160:163], v[104:107]\n"
    "ds_read_b64_tr_b16 v[18:19], v6 offset:2048\n"
    "s_mov_b32 exec_lo, 3\n"
    "s_mov_b32 exec_hi, 3\n"
    "ds_write2_b32 v42, v244, v246 offset0:64 offset1:65\n"
    "ds_write2_b32 v42, v43, v43 offset0:68 offset1:69\n"
    "s_mov_b64 exec, -1\n"
    "s_waitcnt lgkmcnt(7)\n"
    "v_mfma_f32_16x16x32_bf16 v[112:115], v[24:27], v[160:163], v[112:115]\n"
    "v_add_u32 v7, 0xffff4000, v7\n"
    "ds_read_b64_tr_b16 v[20:21], v7 offset:0\n"
    "v_mfma_f32_16x16x32_bf16 v[116:119], v[24:27], v[164:167], v[116:119]\n"
    "ds_read_b64_tr_b16 v[22:23], v7 offset:2048\n"
    "s_waitcnt lgkmcnt(7)\n"
    "v_mfma_f32_16x16x32_bf16 v[124:127], v[28:31], v[164:167], v[124:127]\n"
    "v_add_u32 v8, 0xffff4000, v8\n"
    "ds_read_b64_tr_b16 v[24:25], v8 offset:0\n"
    "v_mfma_f32_16x16x32_bf16 v[120:123], v[28:31], v[160:163], v[120:123]\n"
    "ds_read_b64_tr_b16 v[26:27], v8 offset:2048\n"
    "s_waitcnt lgkmcnt(6)\n"
    "v_mfma_f32_16x16x32_bf16 v[96:99], v[16:19], v[168:171], v[96:99]\n"
    "v_add_u32 v4, 0xffff4000, v4\n"
    "ds_read_b64_tr_b16 v[28:29], v4 offset:0\n"
    "v_mfma_f32_16x16x32_bf16 v[100:103], v[16:19], v[172:175], v[100:103]\n"
    "ds_read_b64_tr_b16 v[30:31], v4 offset:2048\n"
    "s_waitcnt lgkmcnt(4)\n"
    "v_mfma_f32_16x16x32_bf16 v[108:111], v[20:23], v[172:175], v[108:111]\n"
    "ds_read_b64_tr_b16 v[16:17], v6 offset:16384\n"
    "v_mfma_f32_16x16x32_bf16 v[104:107], v[20:23], v[168:171], v[104:107]\n"
    "ds_read_b64_tr_b16 v[18:19], v6 offset:18432\n"
    "s_waitcnt lgkmcnt(4)\n"
    "v_mfma_f32_16x16x32_bf16 v[112:115], v[24:27], v[168:171], v[112:115]\n"
    "ds_read_b64_tr_b16 v[20:21], v7 offset:16384\n"
    "v_mfma_f32_16x16x32_bf16 v[116:119], v[24:27], v[172:175], v[116:119]\n"
    "ds_read_b64_tr_b16 v[22:23], v7 offset:18432\n"
    "s_waitcnt lgkmcnt(4)\n"
    "v_mfma_f32_16x16x32_bf16 v[124:127], v[28:31], v[172:175], v[124:127]\n"
    "ds_read_b64_tr_b16 v[24:25], v8 offset:16384\n"
    "v_mfma_f32_16x16x32_bf16 v[120:123], v[28:31], v[168:171], v[120:123]\n"
    "ds_read_b64_tr_b16 v[26:27], v8 offset:18432\n"
    "s_cbranch_vccz .Lfz_RET_bX47\n"
    "s_barrier\n"
    ".Lfz_RET_bX47:\n"
    "s_add_u32 m0, s52, 0x10000\n"
    "s_mov_b32 s54, 0x120000\n"
    "buffer_load_dwordx4 v9, s[36:39], s54 offen lds\n"
    "s_add_u32 m0, s52, 0x12000\n"
    "s_mov_b32 s55, 0x130000\n"
    "buffer_load_dwordx4 v9, s[36:39], s55 offen lds\n"
    "s_add_u32 m0, s52, 0x14000\n"
    "s_mov_b32 s54, 0x140000\n"
    "buffer_load_dwordx4 v9, s[36:39], s54 offen lds\n"
    "s_add_u32 m0, s52, 0x16000\n"
    "s_mov_b32 s55, 0x150000\n"
    "buffer_load_dwordx4 v9, s[36:39], s55 offen lds\n"
    "s_mov_b32 s88, 0xe000\n"
    "buffer_load_dwordx4 v[200:203], v11, s[44:47], s88 offen offset:0 sc1\n"
    "s_mov_b32 s88, 0xe000\n"
    "buffer_load_dwordx4 v[204:207], v11, s[44:47], s88 offen offset:1024 sc1\n"
    "s_cbranch_vccnz .Lfz_RET_bX48\n"
    "s_barrier\n"
    ".Lfz_RET_bX48:\n"
    "s_waitcnt vmcnt(11) lgkmcnt(4)\n"
    "v_mfma_f32_16x16x32_bf16 v[96:99], v[16:19], v[176:179], v[96:99]\n"
    "ds_read_b64_tr_b16 v[28:29], v4 offset:16384\n"
    "s_waitcnt vmcnt(10)\n"
    "v_mfma_f32_16x16x32_bf16 v[100:103], v[16:19], v[180:183], v[100:103]\n"
    "ds_read_b64_tr_b16 v[30:31], v4 offset:18432\n"
    "ds_read_b128 v[240:243], v45 offset:0\n"
    "s_waitcnt lgkmcnt(5)\n"
    "v_mfma_f32_16x16x32_bf16 v[108:111], v[20:23], v[180:183], v[108:111]\n"
    "v_add_u32 v6, 0x18000, v6\n"
    "ds_read_b64_tr_b16 v[16:17], v6 offset:0\n"
    "v_mfma_f32_16x16x32_bf16 v[104:107], v[20:23], v[176:179], v[104:107]\n"
    "ds_read_b64_tr_b16 v[18:19], v6 offset:2048\n"
    "ds_read_b128 v[248:251], v47 offset:0\n"
    "s_waitcnt lgkmcnt(6)\n"
    "v_mfma_f32_16x16x32_bf16 v[112:115], v[24:27], v[176:179], v[112:115]\n"
    "v_add_u32 v7, 0x18000, v7\n"
    "ds_read_b64_tr_b16 v[20:21], v7 offset:0\n"
    "v_mfma_f32_16x16x32_bf16 v[116:119], v[24:27], v[180:183], v[116:119]\n"
    "ds_read_b64_tr_b16 v[22:23], v7 offset:2048\n"
    "ds_read_b128 v[48:51], v46 offset:0\n"
    "s_waitcnt lgkmcnt(7)\n"
    "v_mfma_f32_16x16x32_bf16 v[124:127], v[28:31], v[180:183], v[124:127]\n"
    "v_add_u32 v8, 0x18000, v8\n"
    "ds_read_b64_tr_b16 v[24:25], v8 offset:0\n"
    "v_mfma_f32_16x16x32_bf16 v[120:123], v[28:31], v[176:179], v[120:123]\n"
    "ds_read_b64_tr_b16 v[26:27], v8 offset:2048\n"
    "ds_read_b128 v[244:247], v45 offset:288\n"
    "s_waitcnt vmcnt(9) lgkmcnt(7)\n"
    "v_mfma_f32_16x16x32_bf16 v[96:99], v[16:19], v[184:187], v[96:99]\n"
    "v_add_u32 v4, 0x18000, v4\n"
    "ds_read_b64_tr_b16 v[28:29], v4 offset:0\n"
    "s_waitcnt vmcnt(8)\n"
    "v_mfma_f32_16x16x32_bf16 v[100:103], v[16:19], v[188:191], v[100:103]\n"
    "ds_read_b64_tr_b16 v[30:31], v4 offset:2048\n"
    "ds_read_b128 v[252:255], v47 offset:288\n"
    "s_waitcnt lgkmcnt(7)\n"
    "v_mfma_f32_16x16x32_bf16 v[108:111], v[20:23], v[188:191], v[108:111]\n"
    "ds_read_b64_tr_b16 v[16:17], v6 offset:16384\n"
    "v_mfma_f32_16x16x32_bf16 v[104:107], v[20:23], v[184:187], v[104:107]\n"
    "ds_read_b64_tr_b16 v[18:19], v6 offset:18432\n"
    "ds_read_b128 v[52:55], v46 offset:288\n"
    "s_waitcnt lgkmcnt(7)\n"
    "v_mfma_f32_16x16x32_bf16 v[112:115], v[24:27], v[184:187], v[112:115]\n"
    "ds_read_b64_tr_b16 v[20:21], v7 offset:16384\n"
    "v_mfma_f32_16x16x32_bf16 v[116:119], v[24:27], v[188:191], v[116:119]\n"
    "ds_read_b64_tr_b16 v[22:23], v7 offset:18432\n"
    "s_waitcnt lgkmcnt(6)\n"
    "v_mfma_f32_16x16x32_bf16 v[124:127], v[28:31], v[188:191], v[124:127]\n"
    "ds_read_b64_tr_b16 v[24:25], v8 offset:16384\n"
    "v_mfma_f32_16x16x32_bf16 v[120:123], v[28:31], v[184:187], v[120:123]\n"
    "ds_read_b64_tr_b16 v[26:27], v8 offset:18432\n"
    "v_mfma_f32_16x16x32_bf16 v[80:83], v[48:51], v[32:35], 0\n"
    "v_mfma_f32_16x16x32_bf16 v[84:87], v[48:51], v[56:59], 0\n"
    "v_mfma_f32_16x16x32_bf16 v[64:67], v[240:243], v[32:35], 0\n"
    "v_mfma_f32_16x16x32_bf16 v[68:71], v[240:243], v[56:59], 0\n"
    "v_mfma_f32_16x16x32_bf16 v[72:75], v[248:251], v[32:35], 0\n"
    "v_mfma_f32_16x16x32_bf16 v[76:79], v[248:251], v[56:59], 0\n"
    "s_waitcnt lgkmcnt(4)\n"
    "v_mfma_f32_16x16x32_bf16 v[80:83], v[52:55], v[36:39], v[80:83]\n"
    "v_mfma_f32_16x16x32_bf16 v[84:87], v[52:55], v[60:63], v[84:87]\n"
    "v_mfma_f32_16x16x32_bf16 v[64:67], v[244:247], v[36:39], v[64:67]\n"
    "v_mfma_f32_16x16x32_bf16 v[68:71], v[244:247], v[60:63], v[68:71]\n"
    "v_mfma_f32_16x16x32_bf16 v[72:75], v[252:255], v[36:39], v[72:75]\n"
    "v_mfma_f32_16x16x32_bf16 v[76:79], v[252:255], v[60:63], v[76:79]\n"
    "s_cbranch_vccz .Lfz_RET_bX49\n"
    "s_barrier\n"
    ".Lfz_RET_bX49:\n"
    "s_add_u32 m0, s52, 0x18000\n"
    "s_mov_b32 s54, 0x160000\n"
    "buffer_load_dwordx4 v9, s[36:39], s54 offen lds\n"
    "v_add_f32 v80, 0x358637bd, v80\n"
    "v_add_f32 v81, 0x358637bd, v81\n"
    "v_add_f32 v84, 0x358637bd, v84\n"
    "v_add_f32 v85, 0x358637bd, v85\n"
    "v_rcp_f32 v80, v80\n"
    "v_rcp_f32 v81, v81\n"
    "v_rcp_f32 v84, v84\n"
    "s_add_u32 m0, s52, 0x1a000\n"
    "s_mov_b32 s55, 0x170000\n"
    "buffer_load_dwordx4 v9, s[36:39], s55 offen lds\n"
    "v_rcp_f32 v85, v85\n"
    "v_mul_f32 v64, v80, v64\n"
    "v_mul_f32 v65, v80, v65\n"
    "v_mul_f32 v66, v80, v66\n"
    "v_mul_f32 v67, v80, v67\n"
    "v_mul_f32 v72, v81, v72\n"
    "v_mul_f32 v73, v81, v73\n"
    "v_mul_f32 v74, v81, v74\n"
    "v_mul_f32 v75, v81, v75\n"
    "s_add_u32 m0, s52, 0x4000\n"
    "s_mov_b32 s54, 0x0\n"
    "buffer_load_dwordx4 v9, s[24:27], s54 offen lds\n"
    "v_mul_f32 v68, v84, v68\n"
    "v_mul_f32 v69, v84, v69\n"
    "v_mul_f32 v70, v84, v70\n"
    "v_mul_f32 v71, v84, v71\n"
    "v_mul_f32 v76, v85, v76\n"
    "v_mul_f32 v77, v85, v77\n"
    "v_mul_f32 v78, v85, v78\n"
    "v_mul_f32 v79, v85, v79\n"
    "v_cvt_pk_bf16_f32 v40, v64, v65\n"
    "v_cvt_pk_bf16_f32 v41, v66, v67\n"
    "s_add_u32 m0, s52, 0x6000\n"
    "s_mov_b32 s55, 0x10000\n"
    "buffer_load_dwordx4 v9, s[24:27], s55 offen lds\n"
    "v_cvt_pk_bf16_f32 v42, v72, v73\n"
    "v_cvt_pk_bf16_f32 v43, v74, v75\n"
    "v_cvt_pk_bf16_f32 v44, v68, v69\n"
    "v_cvt_pk_bf16_f32 v45, v70, v71\n"
    "v_cvt_pk_bf16_f32 v46, v76, v77\n"
    "v_cvt_pk_bf16_f32 v47, v78, v79\n"
    "buffer_store_dwordx4 v[40:43], v10, s[44:47], s69 offen offset:0 sc1\n"
    "buffer_store_dwordx4 v[44:47], v10, s[44:47], s69 offen offset:1024 sc1\n"
    "v_mov_b32 v14, 0\n"
    "s_mov_b64 exec, 1\n"
    "buffer_atomic_or v14, off, s[44:47], s6 offset:12 sc0\n"
    "s_mov_b64 exec, -1\n"
    "s_cbranch_vccnz .Lfz_RET_bX50\n"
    "s_barrier\n"
    ".Lfz_RET_bX50:\n"
    "s_waitcnt vmcnt(14)\n"
    "v_mfma_f32_16x16x32_bf16 v[96:99], v[16:19], v[192:195], v[96:99]\n"
    "ds_read_b64_tr_b16 v[28:29], v4 offset:16384\n"
    "s_waitcnt vmcnt(13)\n"
    "v_mfma_f32_16x16x32_bf16 v[100:103], v[16:19], v[196:199], v[100:103]\n"
    "ds_read_b64_tr_b16 v[30:31], v4 offset:18432\n"
    "s_waitcnt lgkmcnt(4)\n"
    "v_mfma_f32_16x16x32_bf16 v[108:111], v[20:23], v[196:199], v[108:111]\n"
    "ds_read_b64_tr_b16 v[16:17], v6 offset:32768\n"
    "v_mfma_f32_16x16x32_bf16 v[104:107], v[20:23], v[192:195], v[104:107]\n"
    "ds_read_b64_tr_b16 v[18:19], v6 offset:34816\n"
    "s_waitcnt lgkmcnt(4)\n"
    "v_mfma_f32_16x16x32_bf16 v[112:115], v[24:27], v[192:195], v[112:115]\n"
    "ds_read_b64_tr_b16 v[20:21], v7 offset:32768\n"
    "v_mfma_f32_16x16x32_bf16 v[116:119], v[24:27], v[196:199], v[116:119]\n"
    "ds_read_b64_tr_b16 v[22:23], v7 offset:34816\n"
    "s_waitcnt lgkmcnt(4)\n"
    "v_mfma_f32_16x16x32_bf16 v[124:127], v[28:31], v[196:199], v[124:127]\n"
    "ds_read_b64_tr_b16 v[24:25], v8 offset:32768\n"
    "v_mfma_f32_16x16x32_bf16 v[120:123], v[28:31], v[192:195], v[120:123]\n"
    "ds_read_b64_tr_b16 v[26:27], v8 offset:34816\n"
    "s_waitcnt vmcnt(8) lgkmcnt(4)\n"
    "v_mfma_f32_16x16x32_bf16 v[96:99], v[16:19], v[200:203], v[96:99]\n"
    "ds_read_b64_tr_b16 v[28:29], v4 offset:32768\n"
    "s_waitcnt vmcnt(7)\n"
    "v_mfma_f32_16x16x32_bf16 v[100:103], v[16:19], v[204:207], v[100:103]\n"
    "ds_read_b64_tr_b16 v[30:31], v4 offset:34816\n"
    "s_waitcnt lgkmcnt(4)\n"
    "v_mfma_f32_16x16x32_bf16 v[108:111], v[20:23], v[204:207], v[108:111]\n"
    "v_add_u32 v6, 0xfffe4000, v6\n"
    "ds_read_b64_tr_b16 v[16:17], v6 offset:0\n"
    "v_mfma_f32_16x16x32_bf16 v[104:107], v[20:23], v[200:203], v[104:107]\n"
    "ds_read_b64_tr_b16 v[18:19], v6 offset:2048\n"
    "s_waitcnt lgkmcnt(4)\n"
    "v_mfma_f32_16x16x32_bf16 v[112:115], v[24:27], v[200:203], v[112:115]\n"
    "v_add_u32 v7, 0xfffe4000, v7\n"
    "ds_read_b64_tr_b16 v[20:21], v7 offset:0\n"
    "v_mfma_f32_16x16x32_bf16 v[116:119], v[24:27], v[204:207], v[116:119]\n"
    "ds_read_b64_tr_b16 v[22:23], v7 offset:2048\n"
    "s_waitcnt lgkmcnt(4)\n"
    "v_mfma_f32_16x16x32_bf16 v[124:127], v[28:31], v[204:207], v[124:127]\n"
    "v_add_u32 v8, 0xfffe4000, v8\n"
    "ds_read_b64_tr_b16 v[24:25], v8 offset:0\n"
    "v_mfma_f32_16x16x32_bf16 v[120:123], v[28:31], v[200:203], v[120:123]\n"
    "ds_read_b64_tr_b16 v[26:27], v8 offset:2048\n"
    "s_cbranch_vccz .Lfz_RET_bX51\n"
    "s_barrier\n"
    ".Lfz_RET_bX51:\n"
    "s_add_u32 m0, s52, 0x8000\n"
    "s_mov_b32 s54, 0x20000\n"
    "buffer_load_dwordx4 v9, s[24:27], s54 offen lds\n"
    "s_waitcnt vmcnt(2)\n"
    "s_mov_b64 exec, 1\n"
    "buffer_atomic_add v12, off, s[44:47], s64 offset:16\n"
    "s_mov_b64 exec, -1\n"
    "s_add_u32 m0, s52, 0xa000\n"
    "s_mov_b32 s55, 0x30000\n"
    "buffer_load_dwordx4 v9, s[24:27], s55 offen lds\n"
    "s_add_u32 m0, s52, 0xc000\n"
    "s_mov_b32 s54, 0x40000\n"
    "buffer_load_dwordx4 v9, s[24:27], s54 offen lds\n"
    "s_add_u32 m0, s52, 0xe000\n"
    "s_mov_b32 s55, 0x50000\n"
    "buffer_load_dwordx4 v9, s[24:27], s55 offen lds\n"
    "s_waitcnt vmcnt(5)\n"
    "v_readfirstlane_b32 s90, v14\n"
    "s_sub_u32 s90, s90, s53\n"
    "s_cmp_eq_u32 s90, 4\n"
    "s_cbranch_scc0 .Lfz_SLOW_X_8\n"
    ".Lfz_RET_X_8:\n"
    "s_mov_b32 s88, 0x10000\n"
    "buffer_load_dwordx4 v[208:211], v11, s[44:47], s88 offen offset:0 sc1\n"
    "s_mov_b32 s88, 0x10000\n"
    "buffer_load_dwordx4 v[212:215], v11, s[44:47], s88 offen offset:1024 sc1\n"
    "s_mov_b32 s88, 0x12000\n"
    "buffer_load_dwordx4 v[216:219], v11, s[44:47], s88 offen offset:0 sc1\n"
    "s_mov_b32 s88, 0x12000\n"
    "buffer_load_dwordx4 v[220:223], v11, s[44:47], s88 offen offset:1024 sc1\n"
    "s_mov_b32 s88, 0x14000\n"
    "buffer_load_dwordx4 v[224:227], v11, s[44:47], s88 offen offset:0 sc1\n"
    "s_mov_b32 s88, 0x14000\n"
    "buffer_load_dwordx4 v[228:231], v11, s[44:47], s88 offen offset:1024 sc1\n"
    "s_cbranch_vccnz .Lfz_RET_bX52\n"
    "s_barrier\n"
    ".Lfz_RET_bX52:\n"
    "s_waitcnt vmcnt(5) lgkmcnt(4)\n"
    "v_mfma_f32_16x16x32_bf16 v[96:99], v[16:19], v[208:211], v[96:99]\n"
    "v_add_u32 v4, 0xfffe4000, v4\n"
    "ds_read_b64_tr_b16 v[28:29], v4 offset:0\n"
    "s_waitcnt vmcnt(4)\n"
    "v_mfma_f32_16x16x32_bf16 v[100:103], v[16:19], v[212:215], v[100:103]\n"
    "ds_read_b64_tr_b16 v[30:31], v4 offset:2048\n"
    "s_waitcnt lgkmcnt(4)\n"
    "v_mfma_f32_16x16x32_bf16 v[108:111], v[20:23], v[212:215], v[108:111]\n"
    "v_add_u32 v6, 0x10000, v6\n"
    "ds_read_b64_tr_b16 v[16:17], v6 offset:0\n"
    "v_mfma_f32_16x16x32_bf16 v[104:107], v[20:23], v[208:211], v[104:107]\n"
    "ds_read_b64_tr_b16 v[18:19], v6 offset:2048\n"
    "s_waitcnt lgkmcnt(4)\n"
    "v_mfma_f32_16x16x32_bf16 v[112:115], v[24:27], v[208:211], v[112:115]\n"
    "v_add_u32 v7, 0x10000, v7\n"
    "ds_read_b64_tr_b16 v[20:21], v7 offset:0\n"
    "v_mfma_f32_16x16x32_bf16 v[116:119], v[24:27], v[212:215], v[116:119]\n"
    "ds_read_b64_tr_b16 v[22:23], v7 offset:2048\n"
    "s_waitcnt lgkmcnt(4)\n"
    "v_mfma_f32_16x16x32_bf16 v[124:127], v[28:31], v[212:215], v[124:127]\n"
    "v_add_u32 v8, 0x10000, v8\n"
    "ds_read_b64_tr_b16 v[24:25], v8 offset:0\n"
    "v_mfma_f32_16x16x32_bf16 v[120:123], v[28:31], v[208:211], v[120:123]\n"
    "ds_read_b64_tr_b16 v[26:27], v8 offset:2048\n"
    "s_waitcnt vmcnt(3) lgkmcnt(4)\n"
    "v_mfma_f32_16x16x32_bf16 v[96:99], v[16:19], v[216:219], v[96:99]\n"
    "v_add_u32 v4, 0x10000, v4\n"
    "ds_read_b64_tr_b16 v[28:29], v4 offset:0\n"
    "s_waitcnt vmcnt(2)\n"
    "v_mfma_f32_16x16x32_bf16 v[100:103], v[16:19], v[220:223], v[100:103]\n"
    "ds_read_b64_tr_b16 v[30:31], v4 offset:2048\n"
    "s_waitcnt lgkmcnt(4)\n"
    "v_mfma_f32_16x16x32_bf16 v[108:111], v[20:23], v[220:223], v[108:111]\n"
    "ds_read_b64_tr_b16 v[16:17], v6 offset:16384\n"
    "v_mfma_f32_16x16x32_bf16 v[104:107], v[20:23], v[216:219], v[104:107]\n"
    "ds_read_b64_tr_b16 v[18:19], v6 offset:18432\n"
    "s_waitcnt lgkmcnt(4)\n"
    "v_mfma_f32_16x16x32_bf16 v[112:115], v[24:27], v[216:219], v[112:115]\n"
    "ds_read_b64_tr_b16 v[20:21], v7 offset:16384\n"
    "v_mfma_f32_16x16x32_bf16 v[116:119], v[24:27], v[220:223], v[116:119]\n"
    "ds_read_b64_tr_b16 v[22:23], v7 offset:18432\n"
    "s_waitcnt lgkmcnt(4)\n"
    "v_mfma_f32_16x16x32_bf16 v[124:127], v[28:31], v[220:223], v[124:127]\n"
    "ds_read_b64_tr_b16 v[24:25], v8 offset:16384\n"
    "v_mfma_f32_16x16x32_bf16 v[120:123], v[28:31], v[216:219], v[120:123]\n"
    "ds_read_b64_tr_b16 v[26:27], v8 offset:18432\n"
    "s_cbranch_vccz .Lfz_RET_bX53\n"
    "s_barrier\n"
    ".Lfz_RET_bX53:\n"
    "s_add_u32 m0, s52, 0x1c000\n"
    "s_mov_b32 s54, 0x60000\n"
    "buffer_load_dwordx4 v9, s[24:27], s54 offen lds\n"
    "v_mov_b32 v14, 0\n"
    "s_mov_b64 exec, 1\n"
    "buffer_atomic_or v14, off, s[44:47], s6 offset:16 sc0\n"
    "s_mov_b64 exec, -1\n"
    "s_add_u32 m0, s52, 0x24000\n"
    "s_lshl_b32 s87, s83, 16\n"
    "buffer_load_dwordx4 v9, s[12:15], s87 offen lds\n"
    "s_lshl_b32 s87, s48, 11\n"
    "v_lshl_add_u32 v1, v0, 4, s87\n"
    "s_add_u32 m0, s52, 0x1e000\n"
    "s_mov_b32 s55, 0x70000\n"
    "buffer_load_dwordx4 v9, s[24:27], s55 offen lds\n"
    "v_and_b32 v32, 15, v0\n"
    "s_lshl_b32 s87, s84, 5\n"
    "v_add_u32 v32, s87, v32\n"
    "v_bfe_u32 v33, v0, 1, 2\n"
    "v_xor_b32 v33, s85, v33\n"
    "s_mov_b32 s88, 0x16000\n"
    "buffer_load_dwordx4 v[232:235], v11, s[44:47], s88 offen offset:0 sc1\n"
    "v_lshrrev_b32 v34, 4, v0\n"
    "v_lshlrev_b32 v34, 3, v34\n"
    "v_lshl_or_b32 v33, v33, 5, v34\n"
    "v_lshl_or_b32 v3, v32, 7, v33\n"
    "s_mov_b32 s88, 0x16000\n"
    "buffer_load_dwordx4 v[236:239], v11, s[44:47], s88 offen offset:1024 sc1\n"
    "v_lshl_or_b32 v36, v32, 10, v34\n"
    "s_lshl_b32 s87, s83, 16\n"
    "s_lshl_b32 s94, s82, 7\n"
    "s_add_u32 s87, s87, s94\n"
    "s_lshl_b32 s94, s85, 5\n"
    "s_add_u32 s87, s87, s94\n"
    "v_add_u32 v36, s87, v36\n"
    "s_cbranch_vccnz .Lfz_RET_bX54\n"
    "s_barrier\n"
    ".Lfz_RET_bX54:\n"
    "s_waitcnt vmcnt(7) lgkmcnt(4)\n"
    "v_mfma_f32_16x16x32_bf16 v[96:99], v[16:19], v[224:227], v[96:99]\n"
    "ds_read_b64_tr_b16 v[28:29], v4 offset:16384\n"
    "s_waitcnt vmcnt(6)\n"
    "v_mfma_f32_16x16x32_bf16 v[100:103], v[16:19], v[228:231], v[100:103]\n"
    "ds_read_b64_tr_b16 v[30:31], v4 offset:18432\n"
    "s_waitcnt lgkmcnt(4)\n"
    "v_mfma_f32_16x16x32_bf16 v[108:111], v[20:23], v[228:231], v[108:111]\n"
    "ds_read_b64_tr_b16 v[16:17], v6 offset:32768\n"
    "v_mfma_f32_16x16x32_bf16 v[104:107], v[20:23], v[224:227], v[104:107]\n"
    "ds_read_b64_tr_b16 v[18:19], v6 offset:34816\n"
    "s_waitcnt lgkmcnt(4)\n"
    "v_mfma_f32_16x16x32_bf16 v[112:115], v[24:27], v[224:227], v[112:115]\n"
    "ds_read_b64_tr_b16 v[20:21], v7 offset:32768\n"
    "v_mfma_f32_16x16x32_bf16 v[116:119], v[24:27], v[228:231], v[116:119]\n"
    "ds_read_b64_tr_b16 v[22:23], v7 offset:34816\n"
    "s_waitcnt lgkmcnt(4)\n"
    "v_mfma_f32_16x16x32_bf16 v[124:127], v[28:31], v[228:231], v[124:127]\n"
    "ds_read_b64_tr_b16 v[24:25], v8 offset:32768\n"
    "v_mfma_f32_16x16x32_bf16 v[120:123], v[28:31], v[224:227], v[120:123]\n"
    "ds_read_b64_tr_b16 v[26:27], v8 offset:34816\n"
    "s_waitcnt vmcnt(1) lgkmcnt(4)\n"
    "v_mfma_f32_16x16x32_bf16 v[96:99], v[16:19], v[232:235], v[96:99]\n"
    "ds_read_b64_tr_b16 v[28:29], v4 offset:32768\n"
    "s_waitcnt vmcnt(0)\n"
    "v_mfma_f32_16x16x32_bf16 v[100:103], v[16:19], v[236:239], v[100:103]\n"
    "ds_read_b64_tr_b16 v[30:31], v4 offset:34816\n"
    "s_waitcnt lgkmcnt(4)\n"
    "v_mfma_f32_16x16x32_bf16 v[108:111], v[20:23], v[236:239], v[108:111]\n"
    "v_add_u32 v6, 0xffff4000, v6\n"
    "ds_read_b64_tr_b16 v[16:17], v6 offset:0\n"
    "v_mfma_f32_16x16x32_bf16 v[104:107], v[20:23], v[232:235], v[104:107]\n"
    "ds_read_b64_tr_b16 v[18:19], v6 offset:2048\n"
    "s_waitcnt lgkmcnt(4)\n"
    "v_mfma_f32_16x16x32_bf16 v[112:115], v[24:27], v[232:235], v[112:115]\n"
    "v_add_u32 v7, 0xffff4000, v7\n"
    "ds_read_b64_tr_b16 v[20:21], v7 offset:0\n"
    "v_mfma_f32_16x16x32_bf16 v[116:119], v[24:27], v[236:239], v[116:119]\n"
    "ds_read_b64_tr_b16 v[22:23], v7 offset:2048\n"
    "s_waitcnt lgkmcnt(4)\n"
    "v_mfma_f32_16x16x32_bf16 v[124:127], v[28:31], v[236:239], v[124:127]\n"
    "v_add_u32 v8, 0xffff4000, v8\n"
    "ds_read_b64_tr_b16 v[24:25], v8 offset:0\n"
    "v_mfma_f32_16x16x32_bf16 v[120:123], v[28:31], v[232:235], v[120:123]\n"
    "ds_read_b64_tr_b16 v[26:27], v8 offset:2048\n"
    "v_xor_b32 v2, 0x800, v1\n"
    "v_add_u32 v2, 0x10000, v2\n"
    "ds_write_b128 v2, v[104:107] offset:0\n"
    "ds_write_b128 v2, v[108:111] offset:1024\n"
    "v_xor_b32 v2, 0x1000, v1\n"
    "v_add_u32 v2, 0x0, v2\n"
    "ds_write_b128 v2, v[112:115] offset:0\n"
    "ds_write_b128 v2, v[116:119] offset:1024\n"
    "v_xor_b32 v2, 0x1800, v1\n"
    "v_add_u32 v2, 0x20000, v2\n"
    "ds_write_b128 v2, v[120:123] offset:0\n"
    "ds_write_b128 v2, v[124:127] offset:1024\n"
    "s_waitcnt lgkmcnt(0)\n"
    "s_cbranch_vccz .Lfz_RET_bX55\n"
    "s_barrier\n"
    ".Lfz_RET_bX55:\n"
    "v_readfirstlane_b32 s90, v14\n"
    "s_sub_u32 s90, s90, s53\n"
    "s_cmp_eq_u32 s90, 4\n"
    "s_cbranch_scc0 .Lfz_SLOW_X_9\n"
    ".Lfz_RET_X_9:\n"
    "s_mov_b32 s88, 0x1c000\n"
    "buffer_load_dwordx4 v[240:243], v11, s[44:47], s88 offen offset:0 sc1\n"
    "s_mov_b32 s88, 0x1c000\n"
    "buffer_load_dwordx4 v[244:247], v11, s[44:47], s88 offen offset:1024 sc1\n"
    "s_mov_b32 s88, 0x1e000\n"
    "buffer_load_dwordx4 v[248:251], v11, s[44:47], s88 offen offset:0 sc1\n"
    "s_mov_b32 s88, 0x1e000\n"
    "buffer_load_dwordx4 v[252:255], v11, s[44:47], s88 offen offset:1024 sc1\n"
    "s_cbranch_vccnz .Lfz_RET_bX56\n"
    "s_barrier\n"
    ".Lfz_RET_bX56:\n"
    "s_waitcnt vmcnt(3)\n"
    "v_mfma_f32_16x16x32_bf16 v[64:67], v[16:19], v[240:243], 0\n"
    "v_add_u32 v4, 0xffff4000, v4\n"
    "ds_read_b64_tr_b16 v[28:29], v4 offset:0\n"
    "s_waitcnt vmcnt(2)\n"
    "v_mfma_f32_16x16x32_bf16 v[68:71], v[16:19], v[244:247], 0\n"
    "ds_read_b64_tr_b16 v[30:31], v4 offset:2048\n"
    "v_add_u32 v1, 0x10000, v1\n"
    "ds_read_b128 v[104:107], v1 offset:0\n"
    "v_mfma_f32_16x16x32_bf16 v[76:79], v[20:23], v[244:247], 0\n"
    "ds_read_b64_tr_b16 v[16:17], v6 offset:16384\n"
    "v_mfma_f32_16x16x32_bf16 v[72:75], v[20:23], v[240:243], 0\n"
    "ds_read_b64_tr_b16 v[18:19], v6 offset:18432\n"
    "ds_read_b128 v[108:111], v1 offset:1024\n"
    "v_mfma_f32_16x16x32_bf16 v[80:83], v[24:27], v[240:243], 0\n"
    "ds_read_b64_tr_b16 v[20:21], v7 offset:16384\n"
    "v_mfma_f32_16x16x32_bf16 v[84:87], v[24:27], v[244:247], 0\n"
    "ds_read_b64_tr_b16 v[22:23], v7 offset:18432\n"
    "v_add_u32 v1, 0xffff0000, v1\n"
    "ds_read_b128 v[112:115], v1 offset:0\n"
    "s_waitcnt lgkmcnt(7)\n"
    "v_mfma_f32_16x16x32_bf16 v[92:95], v[28:31], v[244:247], 0\n"
    "ds_read_b64_tr_b16 v[24:25], v8 offset:16384\n"
    "v_mfma_f32_16x16x32_bf16 v[88:91], v[28:31], v[240:243], 0\n"
    "ds_read_b64_tr_b16 v[26:27], v8 offset:18432\n"
    "ds_read_b128 v[116:119], v1 offset:1024\n"
    "s_waitcnt vmcnt(1) lgkmcnt(7)\n"
    "v_mfma_f32_16x16x32_bf16 v[64:67], v[16:19], v[248:251], v[64:67]\n"
    "ds_read_b64_tr_b16 v[28:29], v4 offset:16384\n"
    "s_waitcnt vmcnt(0)\n"
    "v_mfma_f32_16x16x32_bf16 v[68:71], v[16:19], v[252:255], v[68:71]\n"
    "ds_read_b64_tr_b16 v[30:31], v4 offset:18432\n"
    "v_add_u32 v1, 0x20000, v1\n"
    "ds_read_b128 v[120:123], v1 offset:0\n"
    "s_waitcnt lgkmcnt(7)\n"
    "v_mfma_f32_16x16x32_bf16 v[76:79], v[20:23], v[252:255], v[76:79]\n"
    "ds_read_b64_tr_b16 v[16:17], v6 offset:32768\n"
    "v_mfma_f32_16x16x32_bf16 v[72:75], v[20:23], v[248:251], v[72:75]\n"
    "ds_read_b64_tr_b16 v[18:19], v6 offset:34816\n"
    "ds_read_b128 v[124:127], v1 offset:1024\n"
    "s_waitcnt lgkmcnt(7)\n"
    "v_mfma_f32_16x16x32_bf16 v[80:83], v[24:27], v[248:251], v[80:83]\n"
    "ds_read_b64_tr_b16 v[20:21], v7 offset:32768\n"
    "v_mfma_f32_16x16x32_bf16 v[84:87], v[24:27], v[252:255], v[84:87]\n"
    "ds_read_b64_tr_b16 v[22:23], v7 offset:34816\n"
    "v_add_u32 v3, 0x24000, v3\n"
    "ds_read_b64 v[32:33], v3 offset:0\n"
    "s_waitcnt lgkmcnt(7)\n"
    "v_mfma_f32_16x16x32_bf16 v[92:95], v[28:31], v[252:255], v[92:95]\n"
    "ds_read_b64_tr_b16 v[24:25], v8 offset:32768\n"
    "v_mfma_f32_16x16x32_bf16 v[88:91], v[28:31], v[248:251], v[88:91]\n"
    "ds_read_b64_tr_b16 v[26:27], v8 offset:34816\n"
    "ds_read_b64 v[34:35], v3 offset:2048\n"
    "s_cbranch_vccz .Lfz_RET_bX57\n"
    "s_barrier\n"
    ".Lfz_RET_bX57:\n"
    "s_mov_b32 s88, 0x20000\n"
    "buffer_load_dwordx4 v[128:131], v11, s[44:47], s88 offen offset:0 sc1\n"
    "v_add_f32 v96, v96, v104\n"
    "v_add_f32 v97, v97, v105\n"
    "v_add_f32 v98, v98, v106\n"
    "v_add_f32 v99, v99, v107\n"
    "v_add_f32 v100, v100, v108\n"
    "v_add_f32 v101, v101, v109\n"
    "v_add_f32 v102, v102, v110\n"
    "v_add_f32 v103, v103, v111\n"
    "v_add_f32 v96, v96, v112\n"
    "v_add_f32 v97, v97, v113\n"
    "v_add_f32 v98, v98, v114\n"
    "v_add_f32 v99, v99, v115\n"
    "v_add_f32 v100, v100, v116\n"
    "v_add_f32 v101, v101, v117\n"
    "v_add_f32 v102, v102, v118\n"
    "v_add_f32 v103, v103, v119\n"
    "s_waitcnt lgkmcnt(9)\n"
    "v_add_f32 v96, v96, v120\n"
    "v_add_f32 v97, v97, v121\n"
    "v_add_f32 v98, v98, v122\n"
    "v_add_f32 v99, v99, v123\n"
    "s_waitcnt lgkmcnt(6)\n"
    "v_add_f32 v100, v100, v124\n"
    "v_add_f32 v101, v101, v125\n"
    "s_mov_b32 s88, 0x20000\n"
    "buffer_load_dwordx4 v[132:135], v11, s[44:47], s88 offen offset:1024 sc1\n"
    "v_add_f32 v102, v102, v126\n"
    "v_add_f32 v103, v103, v127\n"
    "v_mul_f32 v104, 0xbfb8aa3b, v96\n"
    "v_mul_f32 v105, 0xbfb8aa3b, v97\n"
    "v_mul_f32 v106, 0xbfb8aa3b, v98\n"
    "v_mul_f32 v107, 0xbfb8aa3b, v99\n"
    "v_mul_f32 v108, 0xbfb8aa3b, v100\n"
    "v_mul_f32 v109, 0xbfb8aa3b, v101\n"
    "v_mul_f32 v110, 0xbfb8aa3b, v102\n"
    "v_mul_f32 v111, 0xbfb8aa3b, v103\n"
    "v_exp_f32 v104, v104\n"
    "v_exp_f32 v105, v105\n"
    "v_exp_f32 v106, v106\n"
    "v_exp_f32 v107, v107\n"
    "v_exp_f32 v108, v108\n"
    "v_exp_f32 v109, v109\n"
    "s_mov_b32 s88, 0x22000\n"
    "buffer_load_dwordx4 v[136:139], v11, s[44:47], s88 offen offset:0 sc1\n"
    "v_exp_f32 v110, v110\n"
    "v_exp_f32 v111, v111\n"
    "v_add_f32 v104, 1.0, v104\n"
    "v_add_f32 v105, 1.0, v105\n"
    "v_add_f32 v106, 1.0, v106\n"
    "v_add_f32 v107, 1.0, v107\n"
    "v_add_f32 v108, 1.0, v108\n"
    "v_add_f32 v109, 1.0, v109\n"
    "v_add_f32 v110, 1.0, v110\n"
    "v_add_f32 v111, 1.0, v111\n"
    "v_rcp_f32 v104, v104\n"
    "v_rcp_f32 v105, v105\n"
    "v_rcp_f32 v106, v106\n"
    "v_rcp_f32 v107, v107\n"
    "v_rcp_f32 v108, v108\n"
    "s_mov_b32 s88, 0x22000\n"
    "buffer_load_dwordx4 v[140:143], v11, s[44:47], s88 offen offset:1024 sc1\n"
    "v_rcp_f32 v109, v109\n"
    "v_rcp_f32 v110, v110\n"
    "v_rcp_f32 v111, v111\n"
    "v_mul_f32 v96, v96, v104\n"
    "v_mul_f32 v97, v97, v105\n"
    "v_mul_f32 v98, v98, v106\n"
    "v_mul_f32 v99, v99, v107\n"
    "v_mul_f32 v100, v100, v108\n"
    "v_mul_f32 v101, v101, v109\n"
    "v_mul_f32 v102, v102, v110\n"
    "v_mul_f32 v103, v103, v111\n"
    "s_waitcnt lgkmcnt(3)\n"
    "v_lshlrev_b32 v112, 16, v32\n"
    "v_and_b32 v113, 0xffff0000, v32\n"
    "v_lshlrev_b32 v114, 16, v33\n"
    "v_and_b32 v115, 0xffff0000, v33\n"
    "s_waitcnt lgkmcnt(0)\n"
    "v_lshlrev_b32 v116, 16, v34\n"
    "v_and_b32 v117, 0xffff0000, v34\n"
    "v_lshlrev_b32 v118, 16, v35\n"
    "v_and_b32 v119, 0xffff0000, v35\n"
    "s_cbranch_vccnz .Lfz_RET_bX58\n"
    "s_barrier\n"
    ".Lfz_RET_bX58:\n"
    "s_waitcnt vmcnt(3)\n"
    "v_mfma_f32_16x16x32_bf16 v[64:67], v[16:19], v[128:131], v[64:67]\n"
    "ds_read_b64_tr_b16 v[28:29], v4 offset:32768\n"
    "s_waitcnt vmcnt(2)\n"
    "v_mfma_f32_16x16x32_bf16 v[68:71], v[16:19], v[132:135], v[68:71]\n"
    "ds_read_b64_tr_b16 v[30:31], v4 offset:34816\n"
    "v_mfma_f32_16x16x32_bf16 v[76:79], v[20:23], v[132:135], v[76:79]\n"
    "v_add_u32 v6, 0x18000, v6\n"
    "ds_read_b64_tr_b16 v[16:17], v6 offset:0\n"
    "v_mfma_f32_16x16x32_bf16 v[72:75], v[20:23], v[128:131], v[72:75]\n"
    "ds_read_b64_tr_b16 v[18:19], v6 offset:2048\n"
    "v_mfma_f32_16x16x32_bf16 v[80:83], v[24:27], v[128:131], v[80:83]\n"
    "v_add_u32 v7, 0x18000, v7\n"
    "ds_read_b64_tr_b16 v[20:21], v7 offset:0\n"
    "v_mfma_f32_16x16x32_bf16 v[84:87], v[24:27], v[132:135], v[84:87]\n"
    "ds_read_b64_tr_b16 v[22:23], v7 offset:2048\n"
    "s_waitcnt lgkmcnt(4)\n"
    "v_mfma_f32_16x16x32_bf16 v[92:95], v[28:31], v[132:135], v[92:95]\n"
    "v_add_u32 v8, 0x18000, v8\n"
    "ds_read_b64_tr_b16 v[24:25], v8 offset:0\n"
    "v_mfma_f32_16x16x32_bf16 v[88:91], v[28:31], v[128:131], v[88:91]\n"
    "ds_read_b64_tr_b16 v[26:27], v8 offset:2048\n"
    "s_waitcnt vmcnt(1) lgkmcnt(4)\n"
    "v_mfma_f32_16x16x32_bf16 v[64:67], v[16:19], v[136:139], v[64:67]\n"
    "v_add_u32 v4, 0x18000, v4\n"
    "ds_read_b64_tr_b16 v[28:29], v4 offset:0\n"
    "s_waitcnt vmcnt(0)\n"
    "v_mfma_f32_16x16x32_bf16 v[68:71], v[16:19], v[140:143], v[68:71]\n"
    "ds_read_b64_tr_b16 v[30:31], v4 offset:2048\n"
    "s_waitcnt lgkmcnt(4)\n"
    "v_mfma_f32_16x16x32_bf16 v[76:79], v[20:23], v[140:143], v[76:79]\n"
    "v_mfma_f32_16x16x32_bf16 v[72:75], v[20:23], v[136:139], v[72:75]\n"
    "s_waitcnt lgkmcnt(2)\n"
    "v_mfma_f32_16x16x32_bf16 v[80:83], v[24:27], v[136:139], v[80:83]\n"
    "v_mfma_f32_16x16x32_bf16 v[84:87], v[24:27], v[140:143], v[84:87]\n"
    "s_waitcnt lgkmcnt(0)\n"
    "v_mfma_f32_16x16x32_bf16 v[92:95], v[28:31], v[140:143], v[92:95]\n"
    "v_mfma_f32_16x16x32_bf16 v[88:91], v[28:31], v[136:139], v[88:91]\n"
    "s_cbranch_vccz .Lfz_RET_bX59\n"
    "s_barrier\n"
    ".Lfz_RET_bX59:\n"
    "v_xor_b32 v2, 0x800, v1\n"
    "v_add_u32 v2, 0xffff8000, v2\n"
    "ds_write_b128 v2, v[72:75] offset:0\n"
    "ds_write_b128 v2, v[76:79] offset:1024\n"
    "v_xor_b32 v2, 0x1000, v1\n"
    "v_add_u32 v2, 0xffff4000, v2\n"
    "ds_write_b128 v2, v[80:83] offset:0\n"
    "ds_write_b128 v2, v[84:87] offset:1024\n"
    "v_xor_b32 v2, 0x1800, v1\n"
    "v_add_u32 v2, 0xfffe8000, v2\n"
    "ds_write_b128 v2, v[88:91] offset:0\n"
    "ds_write_b128 v2, v[92:95] offset:1024\n"
    "s_waitcnt lgkmcnt(0)\n"
    "s_barrier\n"
    "v_add_u32 v1, 0xffff8000, v1\n"
    "ds_read_b128 v[72:75], v1 offset:0\n"
    "ds_read_b128 v[76:79], v1 offset:1024\n"
    "v_add_u32 v1, 0xffffc000, v1\n"
    "ds_read_b128 v[80:83], v1 offset:0\n"
    "ds_read_b128 v[84:87], v1 offset:1024\n"
    "v_add_u32 v1, 0xffff4000, v1\n"
    "ds_read_b128 v[88:91], v1 offset:0\n"
    "ds_read_b128 v[92:95], v1 offset:1024\n"
    "s_waitcnt lgkmcnt(5)\n"
    "v_pk_add_f32 v[64:65], v[64:65], v[72:73]\n"
    "v_pk_add_f32 v[66:67], v[66:67], v[74:75]\n"
    "s_waitcnt lgkmcnt(4)\n"
    "v_pk_add_f32 v[68:69], v[68:69], v[76:77]\n"
    "v_pk_add_f32 v[70:71], v[70:71], v[78:79]\n"
    "s_waitcnt lgkmcnt(3)\n"
    "v_pk_add_f32 v[64:65], v[64:65], v[80:81]\n"
    "v_pk_add_f32 v[66:67], v[66:67], v[82:83]\n"
    "s_waitcnt lgkmcnt(2)\n"
    "v_pk_add_f32 v[68:69], v[68:69], v[84:85]\n"
    "v_pk_add_f32 v[70:71], v[70:71], v[86:87]\n"
    "s_waitcnt lgkmcnt(1)\n"
    "v_pk_add_f32 v[64:65], v[64:65], v[88:89]\n"
    "v_pk_add_f32 v[66:67], v[66:67], v[90:91]\n"
    "s_waitcnt lgkmcnt(0)\n"
    "v_pk_add_f32 v[68:69], v[68:69], v[92:93]\n"
    "v_pk_add_f32 v[70:71], v[70:71], v[94:95]\n"
    "v_pk_fma_f32 v[64:65], v[64:65], v[96:97], v[112:113]\n"
    "v_pk_fma_f32 v[66:67], v[66:67], v[98:99], v[114:115]\n"
    "v_pk_fma_f32 v[68:69], v[68:69], v[100:101], v[116:117]\n"
    "v_pk_fma_f32 v[70:71], v[70:71], v[102:103], v[118:119]\n"
    "v_cvt_pk_bf16_f32 v32, v64, v65\n"
    "v_cvt_pk_bf16_f32 v33, v66, v67\n"
    "v_cvt_pk_bf16_f32 v34, v68, v69\n"
    "v_cvt_pk_bf16_f32 v35, v70, v71\n"
    "s_movk_i32 s87, 0x4000\n"
    "buffer_store_dwordx2 v[32:33], v36, s[40:43], 0 offen\n"
    "buffer_store_dwordx2 v[34:35], v36, s[40:43], s87 offen\n"
    "s_cbranch_vccnz .Lfz_RET_bX60\n"
    "s_barrier\n"
    ".Lfz_RET_bX60:\n"
    "s_or_b32 s87, %2, s48\n"
    "s_cmp_eq_u32 s87, 0\n"
    "s_mov_b32 s94, 0xd68000\n"
    "s_cselect_b32 s87, s94, 0x40000000\n"
    "s_mov_b64 exec, 1\n"
    "buffer_store_dword v13, off, s[44:47], s87 sc1\n"
    "s_mov_b64 exec, -1\n"
    "s_endpgm\n"
    ".Lfz_SLOW_X_0:\n"
    ".Lfz_SLOW_lX_0:\n"
    "s_cmp_eq_u32 s89, 0\n"
    "s_cbranch_scc1 .Lfz_RET_X_0\n"
    "s_sub_u32 s89, s89, 1\n"
    "s_sleep 1\n"
    "v_mov_b32 v14, 0\n"
    "s_mov_b64 exec, 3\n"
    "buffer_atomic_or v14, v53, s[44:47], s11 offen sc0\n"
    "s_mov_b64 exec, -1\n"
    "s_waitcnt vmcnt(0)\n"
    "v_cmp_eq_u32 s[90:91], s53, v14\n"
    "s_nop 1\n"
    "s_and_b32 s90, s90, 3\n"
    "s_cmp_eq_u32 s90, 3\n"
    "s_cbranch_scc0 .Lfz_SLOW_lX_0\n"
    "s_branch .Lfz_RET_X_0\n"
    ".Lfz_SLOW_X_1:\n"
    "s_mov_b32 s88, 64\n"
    ".Lfz_SLOW_lX_1:\n"
    "s_cmp_eq_u32 s88, 0\n"
    "s_cbranch_scc1 .Lfz_RET_X_1\n"
    "s_sub_u32 s88, s88, 1\n"
    "s_sleep 1\n"
    "v_mov_b32 v3, 0\n"
    "s_mov_b64 exec, 1\n"
    "buffer_atomic_or v3, off, s[44:47], s70 offset:0 sc0\n"
    "s_mov_b64 exec, -1\n"
    "s_waitcnt vmcnt(0)\n"
    "v_readfirstlane_b32 s90, v3\n"
    "s_nop 1\n"
    "s_sub_u32 s90, s90, s53\n"
    "s_cmp_eq_u32 s90, 16\n"
    "s_cbranch_scc0 .Lfz_SLOW_lX_1\n"
    "s_branch .Lfz_RET_X_1\n"
    ".Lfz_SLOW_X_2:\n"
    ".Lfz_SLOW_lX_2:\n"
    "s_cmp_eq_u32 s89, 0\n"
    "s_cbranch_scc1 .Lfz_RET_X_2\n"
    "s_sub_u32 s89, s89, 1\n"
    "s_sleep 1\n"
    "buffer_load_dwordx4 v[80:83], v41, s[44:47], s71 offen sc1\n"
    "buffer_load_dwordx4 v[84:87], v41, s[44:47], s72 offen sc1\n"
    "buffer_load_dwordx4 v[88:91], v42, s[44:47], s73 offen sc1\n"
    "s_waitcnt vmcnt(0)\n"
    "v_cmp_eq_u32 s[90:91], s53, v81\n"
    "s_nop 1\n"
    "v_cmp_eq_u32 s[92:93], s53, v83\n"
    "s_nop 1\n"
    "s_and_b64 s[90:91], s[90:91], s[92:93]\n"
    "v_cmp_eq_u32 s[92:93], s53, v85\n"
    "s_nop 1\n"
    "s_and_b64 s[90:91], s[90:91], s[92:93]\n"
    "v_cmp_eq_u32 s[92:93], s53, v87\n"
    "s_nop 1\n"
    "s_and_b64 s[90:91], s[90:91], s[92:93]\n"
    "v_cmp_eq_u32 s[92:93], s79, v89\n"
    "s_nop 1\n"
    "s_and_b64 s[90:91], s[90:91], s[92:93]\n"
    "v_cmp_eq_u32 s[92:93], s79, v91\n"
    "s_nop 1\n"
    "s_and_b64 s[90:91], s[90:91], s[92:93]\n"
    "s_cmp_eq_u64 s[90:91], -1\n"
    "s_cbranch_scc0 .Lfz_SLOW_lX_2\n"
    "s_branch .Lfz_RET_X_2\n"
    ".Lfz_SLOW_X_3:\n"
    ".Lfz_SLOW_lX_3:\n"
    "s_cmp_eq_u32 s89, 0\n"
    "s_cbranch_scc1 .Lfz_RET_X_3\n"
    "s_sub_u32 s89, s89, 1\n"
    "s_sleep 1\n"
    "v_mov_b32 v14, 0\n"
    "s_mov_b64 exec, 1\n"
    "buffer_atomic_or v14, off, s[44:47], s6 offset:0 sc0\n"
    "s_mov_b64 exec, -1\n"
    "s_waitcnt vmcnt(0)\n"
    "v_readfirstlane_b32 s90, v14\n"
    "s_nop 1\n"
    "s_sub_u32 s90, s90, s53\n"
    "s_cmp_eq_u32 s90, 4\n"
    "s_cbranch_scc0 .Lfz_SLOW_lX_3\n"
    "s_branch .Lfz_RET_X_3\n"
    ".Lfz_SLOW_X_4:\n"
    ".Lfz_SLOW_lX_4:\n"
    "s_cmp_eq_u32 s89, 0\n"
    "s_cbranch_scc1 .Lfz_RET_X_4\n"
    "s_sub_u32 s89, s89, 1\n"
    "s_sleep 1\n"
    "v_mov_b32 v14, 0\n"
    "s_mov_b64 exec, 1\n"
    "buffer_atomic_or v14, off, s[44:47], s6 offset:4 sc0\n"
    "s_mov_b64 exec, -1\n"
    "s_waitcnt vmcnt(0)\n"
    "v_readfirstlane_b32 s90, v14\n"
    "s_nop 1\n"
    "s_sub_u32 s90, s90, s53\n"
    "s_cmp_eq_u32 s90, 4\n"
    "s_cbranch_scc0 .Lfz_SLOW_lX_4\n"
    "s_branch .Lfz_RET_X_4\n"
    ".Lfz_SLOW_X_5:\n"
    "s_mov_b32 s88, 64\n"
    ".Lfz_SLOW_lX_5:\n"
    "s_cmp_eq_u32 s88, 0\n"
    "s_cbranch_scc1 .Lfz_RET_X_5\n"
    "s_sub_u32 s88, s88, 1\n"
    "s_sleep 1\n"
    "v_mov_b32 v3, 0\n"
    "s_mov_b64 exec, 1\n"
    "buffer_atomic_or v3, off, s[44:47], s77 offset:0 sc0\n"
    "s_mov_b64 exec, -1\n"
    "s_waitcnt vmcnt(0)\n"
    "v_readfirstlane_b32 s90, v3\n"
    "s_nop 1\n"
    "s_sub_u32 s90, s90, s53\n"
    "s_cmp_eq_u32 s90, 16\n"
    "s_cbranch_scc0 .Lfz_SLOW_lX_5\n"
    "s_branch .Lfz_RET_X_5\n"
    ".Lfz_SLOW_X_6:\n"
    ".Lfz_SLOW_lX_6:\n"
    "s_cmp_eq_u32 s89, 0\n"
    "s_cbranch_scc1 .Lfz_RET_X_6\n"
    "s_sub_u32 s89, s89, 1\n"
    "s_sleep 1\n"
    "buffer_load_dwordx4 v[240:243], v44, s[44:47], s78 offen sc1\n"
    "s_mov_b32 exec_lo, 3\n"
    "s_mov_b32 exec_hi, 3\n"
    "buffer_load_dwordx4 v[244:247], v44, s[44:47], s78 offen offset:512 sc1\n"
    "s_mov_b64 exec, -1\n"
    "s_waitcnt vmcnt(0)\n"
    "v_cmp_eq_u32 s[90:91], s53, v245\n"
    "s_nop 1\n"
    "v_cmp_eq_u32 s[92:93], s53, v247\n"
    "s_nop 1\n"
    "s_and_b64 s[90:91], s[90:91], s[92:93]\n"
    "s_or_b32 s90, s90, -4\n"
    "s_or_b32 s91, s91, -4\n"
    "v_cmp_eq_u32 s[92:93], s53, v241\n"
    "s_nop 1\n"
    "s_and_b64 s[90:91], s[90:91], s[92:93]\n"
    "v_cmp_eq_u32 s[92:93], s53, v243\n"
    "s_nop 1\n"
    "s_and_b64 s[90:91], s[90:91], s[92:93]\n"
    "s_cmp_eq_u64 s[90:91], -1\n"
    "s_cbranch_scc0 .Lfz_SLOW_lX_6\n"
    "s_branch .Lfz_RET_X_6\n"
    ".Lfz_SLOW_X_7:\n"
    ".Lfz_SLOW_lX_7:\n"
    "s_cmp_eq_u32 s89, 0\n"
    "s_cbranch_scc1 .Lfz_RET_X_7\n"
    "s_sub_u32 s89, s89, 1\n"
    "s_sleep 1\n"
    "v_mov_b32 v14, 0\n"
    "s_mov_b64 exec, 1\n"
    "buffer_atomic_or v14, off, s[44:47], s6 offset:8 sc0\n"
    "s_mov_b64 exec, -1\n"
    "s_waitcnt vmcnt(0)\n"
    "v_readfirstlane_b32 s90, v14\n"
    "s_nop 1\n"
    "s_sub_u32 s90, s90, s53\n"
    "s_cmp_eq_u32 s90, 4\n"
    "s_cbranch_scc0 .Lfz_SLOW_lX_7\n"
    "s_branch .Lfz_RET_X_7\n"
    ".Lfz_SLOW_X_8:\n"
    ".Lfz_SLOW_lX_8:\n"
    "s_cmp_eq_u32 s89, 0\n"
    "s_cbranch_scc1 .Lfz_RET_X_8\n"
    "s_sub_u32 s89, s89, 1\n"
    "s_sleep 1\n"
    "v_mov_b32 v14, 0\n"
    "s_mov_b64 exec, 1\n"
    "buffer_atomic_or v14, off, s[44:47], s6 offset:12 sc0\n"
    "s_mov_b64 exec, -1\n"
    "s_waitcnt vmcnt(0)\n"
    "v_readfirstlane_b32 s90, v14\n"
    "s_nop 1\n"
    "s_sub_u32 s90, s90, s53\n"
    "s_cmp_eq_u32 s90, 4\n"
    "s_cbranch_scc0 .Lfz_SLOW_lX_8\n"
    "s_branch .Lfz_RET_X_8\n"
    ".Lfz_SLOW_X_9:\n"
    ".Lfz_SLOW_lX_9:\n"
    "s_cmp_eq_u32 s89, 0\n"
    "s_cbranch_scc1 .Lfz_RET_X_9\n"
    "s_sub_u32 s89, s89, 1\n"
    "s_sleep 1\n"
    "v_mov_b32 v14, 0\n"
    "s_mov_b64 exec, 1\n"
    "buffer_atomic_or v14, off, s[44:47], s6 offset:16 sc0\n"
    "s_mov_b64 exec, -1\n"
    "s_waitcnt vmcnt(0)\n"
    "v_readfirstlane_b32 s90, v14\n"
    "s_nop 1\n"
    "s_sub_u32 s90, s90, s53\n"
    "s_cmp_eq_u32 s90, 4\n"
    "s_cbranch_scc0 .Lfz_SLOW_lX_9\n"
    "s_branch .Lfz_RET_X_9\n"
    ".Lfz_PATH_Y:\n"
    "s_add_u32 m0, s52, 0x0\n"
    "s_mul_i32 s54, s56, 0x30000\n"
    "s_add_u32 s54, s54, 0x400\n"
    "buffer_load_dwordx4 v7, s[16:19], s54 offen lds\n"
    "s_add_u32 m0, s52, 0x2000\n"
    "s_mul_i32 s55, s56, 0x30000\n"
    "s_add_u32 s55, s55, 0x800\n"
    "buffer_load_dwordx4 v7, s[16:19], s55 offen lds\n"
    "s_add_u32 m0, s52, 0x24000\n"
    "s_lshl_b32 s87, s50, 1\n"
    "s_add_u32 s87, s87, s84\n"
    "s_lshl_b32 s87, s87, 10\n"
    "buffer_load_dwordx4 v33, s[20:23], s87 offen lds\n"
    "v_mov_b32 v14, 0\n"
    "s_mov_b64 exec, 3\n"
    "buffer_atomic_or v14, v53, s[44:47], s11 offen sc0\n"
    "s_mov_b64 exec, -1\n"
    "s_lshl_b32 s83, s88, 2\n"
    "s_and_b32 s87, s98, 3\n"
    "s_add_u32 s83, s83, s87\n"
    "s_lshr_b32 s82, s98, 2\n"
    "s_mov_b32 s89, 0x8000\n"
    "s_mov_b32 s4, 0xff00ff00\n"
    "s_mov_b32 s5, 0xff00ff00\n"
    "s_lshl_b32 s86, s49, 17\n"
    "s_add_u32 s86, s86, 0xb68000\n"
    "v_mov_b32 v12, 1\n"
    "v_bfe_u32 v41, v0, 2, 2\n"
    "v_and_b32 v42, 3, v0\n"
    "v_lshlrev_b32 v42, 3, v42\n"
    "v_lshl_add_u32 v43, v40, 3, v41\n"
    "v_bfe_u32 v45, v0, 3, 2\n"
    "s_lshl_b32 s87, s84, 1\n"
    "v_xor_b32 v45, s87, v45\n"
    "v_lshl_or_b32 v45, v45, 5, v42\n"
    "v_lshl_or_b32 v1, v43, 7, v45\n"
    "v_xor_b32 v2, 32, v1\n"
    "s_setprio 1\n"
    "v_mov_b32 v76, 0\n"
    "v_mov_b32 v78, 0\n"
    "s_waitcnt vmcnt(5)\n"
    "v_dot2c_f32_bf16 v76, s99, v64\n"
    "v_dot2c_f32_bf16 v78, v64, v64\n"
    "v_dot2c_f32_bf16 v76, s99, v65\n"
    "v_dot2c_f32_bf16 v78, v65, v65\n"
    "v_dot2c_f32_bf16 v76, s99, v66\n"
    "v_dot2c_f32_bf16 v78, v66, v66\n"
    "v_dot2c_f32_bf16 v76, s99, v67\n"
    "v_dot2c_f32_bf16 v78, v67, v67\n"
    "v_lshlrev_b32 v68, 16, v64\n"
    "v_and_b32 v69, 0xffff0000, v64\n"
    "v_lshlrev_b32 v70, 16, v65\n"
    "v_permlane32_swap_b32 v76, v78\n"
    "v_add_f32 v76, v76, v78\n"
    "v_and_b32 v71, 0xffff0000, v65\n"
    "v_lshlrev_b32 v72, 16, v66\n"
    "v_add_f32_dpp v76, v76, v76 row_ror:8 row_mask:0xf bank_mask:0xf\n"
    "v_and_b32 v73, 0xffff0000, v66\n"
    "v_lshlrev_b32 v74, 16, v67\n"
    "v_add_f32_dpp v76, v76, v76 row_ror:4 row_mask:0xf bank_mask:0xf\n"
    "v_and_b32 v75, 0xffff0000, v67\n"
    "s_waitcnt vmcnt(4)\n"
    "v_readfirstlane_b32 s53, v13\n"
    "v_add_f32_dpp v76, v76, v76 row_ror:2 row_mask:0xf bank_mask:0xf\n"
    "s_mul_i32 s53, s53, 1664525\n"
    "s_add_u32 s53, s53, 1013904223\n"
    "v_add_f32_dpp v76, v76, v76 row_ror:1 row_mask:0xf bank_mask:0xf\n"
    "v_mov_b32 v78, v76\n"
    "v_mov_b32 v13, s53\n"
    "s_nop 0\n"
    "v_permlane16_swap_b32 v76, v78\n"
    "v_add_f32 v76, v76, v78\n"
    "v_mul_f32 v76, 0x3b000000, v76\n"
    "v_mov_b32 v78, v76\n"
    "s_nop 1\n"
    "v_permlane32_swap_b32 v76, v78\n"
    "v_fma_f32 v78, -v76, v76, v78\n"
    "v_max_f32 v78, 0, v78\n"
    "v_cvt_pk_bf16_f32 v77, v76, v78\n"
    "v_and_b32 v78, 0xffff0000, v77\n"
    "v_lshlrev_b32 v76, 16, v77\n"
    "v_add_f32 v78, 0x3727c5ac, v78\n"
    "v_pk_add_f32 v[68:69], v[68:69], v[76:77] op_sel_hi:[1,0] neg_lo:[0,1] neg_hi:[0,1]\n"
    "v_cvt_pk_bf16_f32 v78, v78, v78\n"
    "v_pk_add_f32 v[70:71], v[70:71], v[76:77] op_sel_hi:[1,0] neg_lo:[0,1] neg_hi:[0,1]\n"
    "v_and_b32 v78, 0xffff0000, v78\n"
    "v_pk_add_f32 v[72:73], v[72:73], v[76:77] op_sel_hi:[1,0] neg_lo:[0,1] neg_hi:[0,1]\n"
    "v_sqrt_f32 v78, v78\n"
    "v_pk_add_f32 v[74:75], v[74:75], v[76:77] op_sel_hi:[1,0] neg_lo:[0,1] neg_hi:[0,1]\n"
    "v_cvt_pk_bf16_f32 v78, v78, v78\n"
    "v_cvt_pk_bf16_f32 v64, v68, v69\n"
    "v_and_b32 v78, 0xffff0000, v78\n"
    "v_cvt_pk_bf16_f32 v65, v70, v71\n"
    "v_rcp_f32 v78, v78\n"
    "v_cvt_pk_bf16_f32 v66, v72, v73\n"
    "v_cvt_pk_bf16_f32 v67, v74, v75\n"
    "v_lshlrev_b32 v68, 16, v64\n"
    "v_and_b32 v69, 0xffff0000, v64\n"
    "v_lshlrev_b32 v70, 16, v65\n"
    "v_and_b32 v71, 0xffff0000, v65\n"
    "v_lshlrev_b32 v72, 16, v66\n"
    "v_and_b32 v73, 0xffff0000, v66\n"
    "v_lshlrev_b32 v74, 16, v67\n"
    "v_and_b32 v75, 0xffff0000, v67\n"
    "v_pk_mul_f32 v[68:69], v[68:69], v[78:79] op_sel_hi:[1,0]\n"
    "v_pk_mul_f32 v[70:71], v[70:71], v[78:79] op_sel_hi:[1,0]\n"
    "v_pk_mul_f32 v[72:73], v[72:73], v[78:79] op_sel_hi:[1,0]\n"
    "v_pk_mul_f32 v[74:75], v[74:75], v[78:79] op_sel_hi:[1,0]\n"
    "v_cvt_pk_bf16_f32 v64, v68, v69\n"
    "v_cvt_pk_bf16_f32 v65, v70, v71\n"
    "v_cvt_pk_bf16_f32 v66, v72, v73\n"
    "v_cvt_pk_bf16_f32 v67, v74, v75\n"
    "buffer_store_dwordx4 v[64:67], v33, s[44:47], s8 offen sc1\n"
    "s_mov_b32 exec_hi, 0\n"
    "buffer_store_dword v13, v54, s[44:47], s80 offen sc1\n"
    "s_mov_b64 exec, -1\n"
    "s_setprio 0\n"
    "s_add_u32 m0, s52, 0x4000\n"
    "s_mul_i32 s54, s57, 0x30000\n"
    "s_add_u32 s54, s54, 0x400\n"
    "buffer_load_dwordx4 v7, s[16:19], s54 offen lds\n"
    "s_add_u32 m0, s52, 0x6000\n"
    "s_mul_i32 s55, s57, 0x30000\n"
    "s_add_u32 s55, s55, 0x800\n"
    "buffer_load_dwordx4 v7, s[16:19], s55 offen lds\n"
    "v_add_u32 v45, s9, v40\n"
    "v_xor_b32 v45, v45, v49\n"
    "v_lshlrev_b32 v45, 4, v45\n"
    "v_lshl_or_b32 v32, v48, 10, v45\n"
    "v_bfe_u32 v45, v0, 1, 3\n"
    "v_xor_b32 v45, v45, v40\n"
    "v_lshlrev_b32 v45, 4, v45\n"
    "v_and_b32 v46, 15, v0\n"
    "v_lshl_or_b32 v45, v46, 7, v45\n"
    "s_lshl_b32 s87, s85, 12\n"
    "v_add_u32 v4, s87, v45\n"
    "v_xor_b32 v5, 64, v4\n"
    "v_lshlrev_b32 v45, 1, v40\n"
    "v_xor_b32 v45, v45, v49\n"
    "v_lshlrev_b32 v45, 4, v45\n"
    "v_lshl_or_b32 v45, v48, 10, v45\n"
    "s_lshl_b32 s87, s82, 7\n"
    "v_add_u32 v9, s87, v45\n"
    "s_lshl_b32 s95, s83, 1\n"
    "s_add_u32 s95, s95, s84\n"
    "s_mul_i32 s87, s95, 0x24000\n"
    "s_lshl_b32 s94, s85, 11\n"
    "s_add_u32 s87, s87, s94\n"
    "v_add_u32 v11, s87, v33\n"
    "s_lshl_b32 s87, s95, 2\n"
    "s_add_u32 s87, s87, s85\n"
    "s_lshl_b32 s87, s87, 7\n"
    "s_add_u32 s6, s87, 0x900000\n"
    "s_lshl_b32 s95, s49, 2\n"
    "s_add_u32 s95, s95, s85\n"
    "s_mul_i32 s87, s95, 0x24000\n"
    "s_lshl_b32 s94, s84, 11\n"
    "s_add_u32 s87, s87, s94\n"
    "v_add_u32 v10, s87, v33\n"
    "s_lshl_b32 s95, s95, 9\n"
    "s_add_u32 s95, s95, 0x900000\n"
    "s_waitcnt vmcnt(2)\n"
    "s_barrier\n"
    "s_mov_b32 exec_hi, 0\n"
    "buffer_store_dword v13, v54, s[44:47], s10 offen sc1\n"
    "s_mov_b64 exec, -1\n"
    "s_add_u32 m0, s52, 0x8000\n"
    "s_mul_i32 s54, s58, 0x30000\n"
    "s_add_u32 s54, s54, 0x400\n"
    "buffer_load_dwordx4 v7, s[16:19], s54 offen lds\n"
    "s_add_u32 m0, s52, 0xa000\n"
    "s_mul_i32 s55, s58, 0x30000\n"
    "s_add_u32 s55, s55, 0x800\n"
    "buffer_load_dwordx4 v7, s[16:19], s55 offen lds\n"
    "s_and_b32 s87, s50, 1\n"
    "s_lshl_b32 s87, s87, 1\n"
    "s_add_u32 s87, s87, s84\n"
    "s_lshl_b32 s87, s87, 7\n"
    "s_add_u32 s64, s87, s95\n"
    "s_lshl_b32 s67, s50, 12\n"
    "v_bfe_u32 v51, v0, 1, 1\n"
    "v_lshlrev_b32 v51, 9, v51\n"
    "v_lshrrev_b32 v45, 2, v0\n"
    "v_lshl_or_b32 v51, v45, 5, v51\n"
    "v_add_u32 v51, s52, v51\n"
    "v_bfe_i32 v52, v0, 2, 1\n"
    "v_and_b32 v52, 0x80008000, v52\n"
    "v_mov_b32 v14, 0\n"
    "s_mov_b64 exec, 3\n"
    "buffer_atomic_or v14, v53, s[44:47], s11 offen sc0\n"
    "s_mov_b64 exec, -1\n"
    "s_add_u32 m0, s52, 0xc000\n"
    "s_mul_i32 s54, s59, 0x30000\n"
    "s_add_u32 s54, s54, 0x400\n"
    "buffer_load_dwordx4 v7, s[16:19], s54 offen lds\n"
    "s_add_u32 m0, s52, 0xe000\n"
    "s_mul_i32 s55, s59, 0x30000\n"
    "s_add_u32 s55, s55, 0x800\n"
    "buffer_load_dwordx4 v7, s[16:19], s55 offen lds\n"
    "s_add_u32 m0, s52, 0x10000\n"
    "s_mul_i32 s54, s60, 0x30000\n"
    "s_add_u32 s54, s54, 0x400\n"
    "buffer_load_dwordx4 v7, s[16:19], s54 offen lds\n"
    "s_add_u32 m0, s52, 0x12000\n"
    "s_mul_i32 s55, s60, 0x30000\n"
    "s_add_u32 s55, s55, 0x800\n"
    "buffer_load_dwordx4 v7, s[16:19], s55 offen lds\n"
    "v_xor_b32 v45, s84, v44\n"
    "v_lshl_or_b32 v45, v45, 5, v42\n"
    "v_lshl_or_b32 v3, v43, 6, v45\n"
    "v_lshrrev_b32 v45, 2, v0\n"
    "s_lshl_b32 s87, s85, 4\n"
    "v_add_u32 v45, s87, v45\n"
    "v_mul_u32_u24 v45, 3584, v45\n"
    "v_and_b32 v46, 3, v0\n"
    "v_and_b32 v47, 2, v40\n"
    "v_xor_b32 v46, v46, v47\n"
    "v_lshl_add_u32 v45, v46, 4, v45\n"
    "s_lshl_b32 s87, s50, 6\n"
    "s_add_u32 s87, s87, 3072\n"
    "v_add_u32 v8, s87, v45\n"
    "s_cmp_eq_u32 s84, 0\n"
    "s_cselect_b32 s70, s56, s57\n"
    "s_cselect_b32 s71, s58, s59\n"
    "s_cselect_b32 s72, s60, s61\n"
    "s_cselect_b32 s73, s62, s63\n"
    "v_add_u32 v51, 0x24000, v51\n"
    "ds_read_b64_tr_b16 v[34:35], v51 offset:0\n"
    "ds_read_b64_tr_b16 v[36:37], v51 offset:8\n"
    "ds_read_b64_tr_b16 v[38:39], v51 offset:16\n"
    "ds_read_b64_tr_b16 v[40:41], v51 offset:24\n"
    "s_waitcnt lgkmcnt(3)\n"
    "v_xor_b32 v34, v52, v34\n"
    "v_xor_b32 v35, v52, v35\n"
    "s_waitcnt lgkmcnt(2)\n"
    "v_xor_b32 v36, v52, v36\n"
    "v_xor_b32 v37, v52, v37\n"
    "s_waitcnt lgkmcnt(1)\n"
    "v_xor_b32 v38, v52, v38\n"
    "v_xor_b32 v39, v52, v39\n"
    "s_waitcnt lgkmcnt(0)\n"
    "v_xor_b32 v40, v52, v40\n"
    "v_xor_b32 v41, v52, v41\n"
    "s_waitcnt vmcnt(4)\n"
    "v_cmp_eq_u32 s[90:91], s53, v14\n"
    "s_and_b32 s90, s90, 3\n"
    "s_cmp_eq_u32 s90, 3\n"
    "s_cbranch_scc0 .Lfz_SLOW_Y_0\n"
    ".Lfz_RET_Y_0:\n"
    "s_add_u32 m0, s52, 0x14000\n"
    "s_lshl_b32 s54, s56, 7\n"
    "s_add_u32 s54, s54, s86\n"
    "buffer_load_dwordx4 v32, s[44:47], s54 offen sc1 lds\n"
    "s_add_u32 m0, s52, 0x16000\n"
    "s_lshl_b32 s55, s56, 7\n"
    "s_add_u32 s55, s55, s86\n"
    "s_add_u32 s55, s55, 0x10000\n"
    "buffer_load_dwordx4 v32, s[44:47], s55 offen sc1 lds\n"
    "s_add_u32 m0, s52, 0x18000\n"
    "s_lshl_b32 s54, s57, 7\n"
    "s_add_u32 s54, s54, s86\n"
    "buffer_load_dwordx4 v32, s[44:47], s54 offen sc1 lds\n"
    "s_add_u32 m0, s52, 0x1a000\n"
    "s_lshl_b32 s55, s57, 7\n"
    "s_add_u32 s55, s55, s86\n"
    "s_add_u32 s55, s55, 0x10000\n"
    "buffer_load_dwordx4 v32, s[44:47], s55 offen sc1 lds\n"
    "s_add_u32 m0, s52, 0x1c000\n"
    "s_lshl_b32 s54, s58, 7\n"
    "s_add_u32 s54, s54, s86\n"
    "buffer_load_dwordx4 v32, s[44:47], s54 offen sc1 lds\n"
    "s_add_u32 m0, s52, 0x1e000\n"
    "s_lshl_b32 s55, s58, 7\n"
    "s_add_u32 s55, s55, s86\n"
    "s_add_u32 s55, s55, 0x10000\n"
    "buffer_load_dwordx4 v32, s[44:47], s55 offen sc1 lds\n"
    "s_waitcnt vmcnt(2)\n"
    "s_cbranch_vccz .Lfz_RET_bY1\n"
    "s_barrier\n"
    ".Lfz_RET_bY1:\n"
    "s_add_u32 m0, s52, 0x20000\n"
    "s_lshl_b32 s54, s59, 7\n"
    "s_add_u32 s54, s54, s86\n"
    "buffer_load_dwordx4 v32, s[44:47], s54 offen sc1 lds\n"
    "s_add_u32 m0, s52, 0x22000\n"
    "s_lshl_b32 s55, s59, 7\n"
    "s_add_u32 s55, s55, s86\n"
    "s_add_u32 s55, s55, 0x10000\n"
    "buffer_load_dwordx4 v32, s[44:47], s55 offen sc1 lds\n"
    "s_add_u32 m0, s52, 0x24000\n"
    "s_lshl_b32 s54, s60, 7\n"
    "s_add_u32 s54, s54, s86\n"
    "buffer_load_dwordx4 v32, s[44:47], s54 offen sc1 lds\n"
    "s_add_u32 m0, s52, 0x26000\n"
    "s_lshl_b32 s55, s60, 7\n"
    "s_add_u32 s55, s55, s86\n"
    "s_add_u32 s55, s55, 0x10000\n"
    "buffer_load_dwordx4 v32, s[44:47], s55 offen sc1 lds\n"
    "s_cbranch_vccnz .Lfz_RET_bY2\n"
    "s_barrier\n"
    ".Lfz_RET_bY2:\n"
    "v_add_u32 v4, 0x14000, v4\n"
    "ds_read_b128 v[128:131], v4 offset:0\n"
    "ds_read_b128 v[132:135], v4 offset:2048\n"
    "v_add_u32 v5, 0x14000, v5\n"
    "ds_read_b128 v[136:139], v5 offset:0\n"
    "ds_read_b128 v[140:143], v5 offset:2048\n"
    "ds_read_b64_tr_b16 v[16:17], v1 offset:0\n"
    "ds_read_b64_tr_b16 v[18:19], v1 offset:512\n"
    "ds_read_b64_tr_b16 v[20:21], v2 offset:0\n"
    "ds_read_b64_tr_b16 v[22:23], v2 offset:512\n"
    "ds_read_b64_tr_b16 v[24:25], v1 offset:8192\n"
    "ds_read_b64_tr_b16 v[26:27], v1 offset:8704\n"
    "s_waitcnt lgkmcnt(4)\n"
    "v_mfma_f32_16x16x32_bf16 v[64:67], v[16:19], v[128:131], 0\n"
    "ds_read_b64_tr_b16 v[28:29], v2 offset:8192\n"
    "v_mfma_f32_16x16x32_bf16 v[68:71], v[16:19], v[132:135], 0\n"
    "ds_read_b64_tr_b16 v[30:31], v2 offset:8704\n"
    "ds_read_b128 v[144:147], v4 offset:16384\n"
    "s_waitcnt lgkmcnt(5)\n"
    "v_mfma_f32_16x16x32_bf16 v[76:79], v[20:23], v[132:135], 0\n"
    "ds_read_b64_tr_b16 v[16:17], v1 offset:4096\n"
    "v_mfma_f32_16x16x32_bf16 v[72:75], v[20:23], v[128:131], 0\n"
    "ds_read_b64_tr_b16 v[18:19], v1 offset:4608\n"
    "ds_read_b128 v[148:151], v4 offset:18432\n"
    "s_waitcnt lgkmcnt(6)\n"
    "v_mfma_f32_16x16x32_bf16 v[80:83], v[24:27], v[128:131], 0\n"
    "ds_read_b64_tr_b16 v[20:21], v2 offset:4096\n"
    "v_mfma_f32_16x16x32_bf16 v[84:87], v[24:27], v[132:135], 0\n"
    "ds_read_b64_tr_b16 v[22:23], v2 offset:4608\n"
    "ds_read_b128 v[152:155], v5 offset:16384\n"
    "s_waitcnt lgkmcnt(7)\n"
    "v_mfma_f32_16x16x32_bf16 v[92:95], v[28:31], v[132:135], 0\n"
    "ds_read_b64_tr_b16 v[24:25], v1 offset:12288\n"
    "v_mfma_f32_16x16x32_bf16 v[88:91], v[28:31], v[128:131], 0\n"
    "ds_read_b64_tr_b16 v[26:27], v1 offset:12800\n"
    "ds_read_b128 v[156:159], v5 offset:18432\n"
    "s_waitcnt lgkmcnt(7)\n"
    "v_mfma_f32_16x16x32_bf16 v[64:67], v[16:19], v[136:139], v[64:67]\n"
    "ds_read_b64_tr_b16 v[28:29], v2 offset:12288\n"
    "v_mfma_f32_16x16x32_bf16 v[68:71], v[16:19], v[140:143], v[68:71]\n"
    "ds_read_b64_tr_b16 v[30:31], v2 offset:12800\n"
    "s_waitcnt lgkmcnt(6)\n"
    "v_mfma_f32_16x16x32_bf16 v[76:79], v[20:23], v[140:143], v[76:79]\n"
    "ds_read_b64_tr_b16 v[16:17], v1 offset:16384\n"
    "v_mfma_f32_16x16x32_bf16 v[72:75], v[20:23], v[136:139], v[72:75]\n"
    "ds_read_b64_tr_b16 v[18:19], v1 offset:16896\n"
    "s_waitcnt lgkmcnt(5)\n"
    "v_mfma_f32_16x16x32_bf16 v[80:83], v[24:27], v[136:139], v[80:83]\n"
    "ds_read_b64_tr_b16 v[20:21], v2 offset:16384\n"
    "v_mfma_f32_16x16x32_bf16 v[84:87], v[24:27], v[140:143], v[84:87]\n"
    "ds_read_b64_tr_b16 v[22:23], v2 offset:16896\n"
    "s_waitcnt lgkmcnt(4)\n"
    "v_mfma_f32_16x16x32_bf16 v[92:95], v[28:31], v[140:143], v[92:95]\n"
    "ds_read_b64_tr_b16 v[24:25], v1 offset:24576\n"
    "v_mfma_f32_16x16x32_bf16 v[88:91], v[28:31], v[136:139], v[88:91]\n"
    "ds_read_b64_tr_b16 v[26:27], v1 offset:25088\n"
    "s_waitcnt vmcnt(4)\n"
    "s_cbranch_vccz .Lfz_RET_bY3\n"
    "s_barrier\n"
    ".Lfz_RET_bY3:\n"
    "s_lshl_b32 s87, %2, 3\n"
    "s_add_u32 s87, s87, s48\n"
    "s_mul_i32 s94, s87, 896\n"
    "v_lshlrev_b32 v112, 7, v0\n"
    "s_mov_b64 exec, 0x7f\n"
    "buffer_load_dword v113, v112, s[36:39], s94 offen\n"
    "s_mov_b64 exec, 3\n"
    "s_lshl_b32 s94, s87, 8\n"
    "buffer_load_dword v114, v112, s[24:27], s94 offen\n"
    "s_mov_b64 exec, -1\n"
    "v_cndmask_b32 v48, v34, 0, s[4:5]\n"
    "v_cndmask_b32 v49, v35, 0, s[4:5]\n"
    "v_cndmask_b32 v50, 0, v34, s[4:5]\n"
    "v_cndmask_b32 v51, 0, v35, s[4:5]\n"
    "v_cndmask_b32 v52, v36, 0, s[4:5]\n"
    "v_cndmask_b32 v53, v37, 0, s[4:5]\n"
    "v_cndmask_b32 v54, 0, v36, s[4:5]\n"
    "v_cndmask_b32 v55, 0, v37, s[4:5]\n"
    "v_cndmask_b32 v56, v38, 0, s[4:5]\n"
    "v_cndmask_b32 v57, v39, 0, s[4:5]\n"
    "v_cndmask_b32 v58, 0, v38, s[4:5]\n"
    "s_cbranch_vccnz .Lfz_RET_bY4\n"
    "s_barrier\n"
    ".Lfz_RET_bY4:\n"
    "s_waitcnt lgkmcnt(4)\n"
    "v_mfma_f32_16x16x32_bf16 v[64:67], v[16:19], v[144:147], v[64:67]\n"
    "ds_read_b64_tr_b16 v[28:29], v2 offset:24576\n"
    "v_mfma_f32_16x16x32_bf16 v[68:71], v[16:19], v[148:151], v[68:71]\n"
    "ds_read_b64_tr_b16 v[30:31], v2 offset:25088\n"
    "ds_read_b128 v[160:163], v4 offset:32768\n"
    "s_waitcnt lgkmcnt(5)\n"
    "v_mfma_f32_16x16x32_bf16 v[76:79], v[20:23], v[148:151], v[76:79]\n"
    "ds_read_b64_tr_b16 v[16:17], v1 offset:20480\n"
    "v_mfma_f32_16x16x32_bf16 v[72:75], v[20:23], v[144:147], v[72:75]\n"
    "ds_read_b64_tr_b16 v[18:19], v1 offset:20992\n"
    "ds_read_b128 v[164:167], v4 offset:34816\n"
    "s_waitcnt lgkmcnt(6)\n"
    "v_mfma_f32_16x16x32_bf16 v[80:83], v[24:27], v[144:147], v[80:83]\n"
    "ds_read_b64_tr_b16 v[20:21], v2 offset:20480\n"
    "v_mfma_f32_16x16x32_bf16 v[84:87], v[24:27], v[148:151], v[84:87]\n"
    "ds_read_b64_tr_b16 v[22:23], v2 offset:20992\n"
    "ds_read_b128 v[168:171], v5 offset:32768\n"
    "s_waitcnt lgkmcnt(7)\n"
    "v_mfma_f32_16x16x32_bf16 v[92:95], v[28:31], v[148:151], v[92:95]\n"
    "ds_read_b64_tr_b16 v[24:25], v1 offset:28672\n"
    "v_mfma_f32_16x16x32_bf16 v[88:91], v[28:31], v[144:147], v[88:91]\n"
    "ds_read_b64_tr_b16 v[26:27], v1 offset:29184\n"
    "ds_read_b128 v[172:175], v5 offset:34816\n"
    "s_waitcnt lgkmcnt(7)\n"
    "v_mfma_f32_16x16x32_bf16 v[64:67], v[16:19], v[152:155], v[64:67]\n"
    "ds_read_b64_tr_b16 v[28:29], v2 offset:28672\n"
    "v_mfma_f32_16x16x32_bf16 v[68:71], v[16:19], v[156:159], v[68:71]\n"
    "ds_read_b64_tr_b16 v[30:31], v2 offset:29184\n"
    "s_waitcnt lgkmcnt(6)\n"
    "v_mfma_f32_16x16x32_bf16 v[76:79], v[20:23], v[156:159], v[76:79]\n"
    "ds_read_b64_tr_b16 v[16:17], v1 offset:32768\n"
    "v_mfma_f32_16x16x32_bf16 v[72:75], v[20:23], v[152:155], v[72:75]\n"
    "ds_read_b64_tr_b16 v[18:19], v1 offset:33280\n"
    "s_waitcnt lgkmcnt(5)\n"
    "v_mfma_f32_16x16x32_bf16 v[80:83], v[24:27], v[152:155], v[80:83]\n"
    "ds_read_b64_tr_b16 v[20:21], v2 offset:32768\n"
    "v_mfma_f32_16x16x32_bf16 v[84:87], v[24:27], v[156:159], v[84:87]\n"
    "ds_read_b64_tr_b16 v[22:23], v2 offset:33280\n"
    "s_waitcnt lgkmcnt(4)\n"
    "v_mfma_f32_16x16x32_bf16 v[92:95], v[28:31], v[156:159], v[92:95]\n"
    "ds_read_b64_tr_b16 v[24:25], v1 offset:40960\n"
    "v_mfma_f32_16x16x32_bf16 v[88:91], v[28:31], v[152:155], v[88:91]\n"
    "ds_read_b64_tr_b16 v[26:27], v1 offset:41472\n"
    "s_waitcnt vmcnt(4)\n"
    "s_cbranch_vccz .Lfz_RET_bY5\n"
    "s_barrier\n"
    ".Lfz_RET_bY5:\n"
    "s_add_u32 m0, s52, 0x0\n"
    "s_lshl_b32 s54, s61, 7\n"
    "s_add_u32 s54, s54, s86\n"
    "buffer_load_dwordx4 v32, s[44:47], s54 offen sc1 lds\n"
    "v_cndmask_b32 v59, 0, v39, s[4:5]\n"
    "v_cndmask_b32 v60, v40, 0, s[4:5]\n"
    "v_cndmask_b32 v61, v41, 0, s[4:5]\n"
    "v_cndmask_b32 v62, 0, v40, s[4:5]\n"
    "v_cndmask_b32 v63, 0, v41, s[4:5]\n"
    "s_add_u32 m0, s52, 0x2000\n"
    "s_lshl_b32 s55, s61, 7\n"
    "s_add_u32 s55, s55, s86\n"
    "s_add_u32 s55, s55, 0x10000\n"
    "buffer_load_dwordx4 v32, s[44:47], s55 offen sc1 lds\n"
    "s_lshl_b32 s87, s50, 3\n"
    "s_add_u32 s87, s87, s48\n"
    "s_lshl_b32 s87, s87, 5\n"
    "s_lshl_b32 s94, s87, 2\n"
    "s_add_u32 s75, s94, 0xb5c000\n"
    "s_add_u32 s87, s87, s49\n"
    "s_mul_i32 s87, s87, 1152\n"
    "s_add_u32 s74, s87, 0x908000\n"
    "s_add_u32 m0, s52, 0x14000\n"
    "s_mul_i32 s54, s61, 0x30000\n"
    "s_add_u32 s54, s54, 0x400\n"
    "buffer_load_dwordx4 v7, s[16:19], s54 offen lds\n"
    "s_and_b32 s87, s50, 3\n"
    "s_lshl_b32 s87, s87, 7\n"
    "s_add_u32 s65, s87, s95\n"
    "s_add_u32 s68, s50, 48\n"
    "s_lshl_b32 s68, s68, 11\n"
    "s_mul_i32 s87, s84, 1536\n"
    "s_sub_u32 s68, s68, s87\n"
    "s_add_u32 s69, s67, 65536\n"
    "s_add_u32 m0, s52, 0x16000\n"
    "s_mul_i32 s55, s61, 0x30000\n"
    "s_add_u32 s55, s55, 0x800\n"
    "buffer_load_dwordx4 v7, s[16:19], s55 offen lds\n"
    "v_and_b32 v34, 15, v0\n"
    "s_lshl_b32 s87, s85, 5\n"
    "v_add_u32 v34, s87, v34\n"
    "v_lshlrev_b32 v34, 6, v34\n"
    "s_cbranch_vccnz .Lfz_RET_bY6\n"
    "s_barrier\n"
    ".Lfz_RET_bY6:\n"
    "s_waitcnt lgkmcnt(4)\n"
    "v_mfma_f32_16x16x32_bf16 v[64:67], v[16:19], v[160:163], v[64:67]\n"
    "ds_read_b64_tr_b16 v[28:29], v2 offset:40960\n"
    "v_mfma_f32_16x16x32_bf16 v[68:71], v[16:19], v[164:167], v[68:71]\n"
    "ds_read_b64_tr_b16 v[30:31], v2 offset:41472\n"
    "ds_read_b128 v[176:179], v4 offset:49152\n"
    "s_waitcnt lgkmcnt(5)\n"
    "v_mfma_f32_16x16x32_bf16 v[76:79], v[20:23], v[164:167], v[76:79]\n"
    "ds_read_b64_tr_b16 v[16:17], v1 offset:36864\n"
    "v_mfma_f32_16x16x32_bf16 v[72:75], v[20:23], v[160:163], v[72:75]\n"
    "ds_read_b64_tr_b16 v[18:19], v1 offset:37376\n"
    "ds_read_b128 v[180:183], v4 offset:51200\n"
    "s_waitcnt lgkmcnt(6)\n"
    "v_mfma_f32_16x16x32_bf16 v[80:83], v[24:27], v[160:163], v[80:83]\n"
    "ds_read_b64_tr_b16 v[20:21], v2 offset:36864\n"
    "v_mfma_f32_16x16x32_bf16 v[84:87], v[24:27], v[164:167], v[84:87]\n"
    "ds_read_b64_tr_b16 v[22:23], v2 offset:37376\n"
    "ds_read_b128 v[184:187], v5 offset:49152\n"
    "s_waitcnt lgkmcnt(7)\n"
    "v_mfma_f32_16x16x32_bf16 v[92:95], v[28:31], v[164:167], v[92:95]\n"
    "ds_read_b64_tr_b16 v[24:25], v1 offset:45056\n"
    "v_mfma_f32_16x16x32_bf16 v[88:91], v[28:31], v[160:163], v[88:91]\n"
    "ds_read_b64_tr_b16 v[26:27], v1 offset:45568\n"
    "ds_read_b128 v[188:191], v5 offset:51200\n"
    "s_waitcnt lgkmcnt(7)\n"
    "v_mfma_f32_16x16x32_bf16 v[64:67], v[16:19], v[168:171], v[64:67]\n"
    "ds_read_b64_tr_b16 v[28:29], v2 offset:45056\n"
    "v_mfma_f32_16x16x32_bf16 v[68:71], v[16:19], v[172:175], v[68:71]\n"
    "ds_read_b64_tr_b16 v[30:31], v2 offset:45568\n"
    "s_waitcnt lgkmcnt(6)\n"
    "v_mfma_f32_16x16x32_bf16 v[76:79], v[20:23], v[172:175], v[76:79]\n"
    "ds_read_b64_tr_b16 v[16:17], v1 offset:49152\n"
    "v_mfma_f32_16x16x32_bf16 v[72:75], v[20:23], v[168:171], v[72:75]\n"
    "ds_read_b64_tr_b16 v[18:19], v1 offset:49664\n"
    "s_waitcnt lgkmcnt(5)\n"
    "v_mfma_f32_16x16x32_bf16 v[80:83], v[24:27], v[168:171], v[80:83]\n"
    "ds_read_b64_tr_b16 v[20:21], v2 offset:49152\n"
    "v_mfma_f32_16x16x32_bf16 v[84:87], v[24:27], v[172:175], v[84:87]\n"
    "ds_read_b64_tr_b16 v[22:23], v2 offset:49664\n"
    "s_waitcnt lgkmcnt(4)\n"
    "v_mfma_f32_16x16x32_bf16 v[92:95], v[28:31], v[172:175], v[92:95]\n"
    "ds_read_b64_tr_b16 v[24:25], v1 offset:57344\n"
    "v_mfma_f32_16x16x32_bf16 v[88:91], v[28:31], v[168:171], v[88:91]\n"
    "ds_read_b64_tr_b16 v[26:27], v1 offset:57856\n"
    "s_waitcnt vmcnt(6)\n"
    "s_cbranch_vccz .Lfz_RET_bY7\n"
    "s_barrier\n"
    ".Lfz_RET_bY7:\n"
    "s_add_u32 m0, s52, 0x4000\n"
    "s_lshl_b32 s54, s62, 7\n"
    "s_add_u32 s54, s54, s86\n"
    "buffer_load_dwordx4 v32, s[44:47], s54 offen sc1 lds\n"
    "v_bfe_u32 v35, v0, 1, 2\n"
    "v_lshrrev_b32 v36, 4, v0\n"
    "v_xor_b32 v38, v36, v35\n"
    "v_lshl_or_b32 v38, v38, 4, v34\n"
    "s_add_u32 m0, s52, 0x6000\n"
    "s_lshl_b32 s55, s62, 7\n"
    "s_add_u32 s55, s55, s86\n"
    "s_add_u32 s55, s55, 0x10000\n"
    "buffer_load_dwordx4 v32, s[44:47], s55 offen sc1 lds\n"
    "v_and_b32 v37, 1, v36\n"
    "v_lshlrev_b32 v37, 1, v37\n"
    "v_xor_b32 v37, v37, v35\n"
    "v_lshl_or_b32 v37, v37, 4, v34\n"
    "s_add_u32 m0, s52, 0x18000\n"
    "s_mul_i32 s54, s62, 0x30000\n"
    "s_add_u32 s54, s54, 0x400\n"
    "buffer_load_dwordx4 v7, s[16:19], s54 offen lds\n"
    "v_lshrrev_b32 v35, 5, v0\n"
    "v_lshl_add_u32 v37, v35, 13, v37\n"
    "v_bfe_u32 v35, v0, 2, 2\n"
    "v_lshl_add_u32 v34, v36, 3, v35\n"
    "s_add_u32 m0, s52, 0x1a000\n"
    "s_mul_i32 s55, s62, 0x30000\n"
    "s_add_u32 s55, s55, 0x800\n"
    "buffer_load_dwordx4 v7, s[16:19], s55 offen lds\n"
    "v_lshlrev_b32 v34, 6, v34\n"
    "v_bfe_u32 v39, v0, 1, 1\n"
    "v_bfe_u32 v35, v0, 3, 1\n"
    "v_xor_b32 v39, v39, v35\n"
    "s_cbranch_vccnz .Lfz_RET_bY8\n"
    "s_barrier\n"
    ".Lfz_RET_bY8:\n"
    "s_waitcnt lgkmcnt(4)\n"
    "v_mfma_f32_16x16x32_bf16 v[64:67], v[16:19], v[176:179], v[64:67]\n"
    "ds_read_b64_tr_b16 v[28:29], v2 offset:57344\n"
    "v_mfma_f32_16x16x32_bf16 v[68:71], v[16:19], v[180:183], v[68:71]\n"
    "ds_read_b64_tr_b16 v[30:31], v2 offset:57856\n"
    "v_add_u32 v4, 0x10000, v4\n"
    "ds_read_b128 v[192:195], v4 offset:0\n"
    "s_waitcnt lgkmcnt(5)\n"
    "v_mfma_f32_16x16x32_bf16 v[76:79], v[20:23], v[180:183], v[76:79]\n"
    "ds_read_b64_tr_b16 v[16:17], v1 offset:53248\n"
    "v_mfma_f32_16x16x32_bf16 v[72:75], v[20:23], v[176:179], v[72:75]\n"
    "ds_read_b64_tr_b16 v[18:19], v1 offset:53760\n"
    "ds_read_b128 v[196:199], v4 offset:2048\n"
    "s_waitcnt lgkmcnt(6)\n"
    "v_mfma_f32_16x16x32_bf16 v[80:83], v[24:27], v[176:179], v[80:83]\n"
    "ds_read_b64_tr_b16 v[20:21], v2 offset:53248\n"
    "v_mfma_f32_16x16x32_bf16 v[84:87], v[24:27], v[180:183], v[84:87]\n"
    "ds_read_b64_tr_b16 v[22:23], v2 offset:53760\n"
    "v_add_u32 v5, 0x10000, v5\n"
    "ds_read_b128 v[200:203], v5 offset:0\n"
    "s_waitcnt lgkmcnt(7)\n"
    "v_mfma_f32_16x16x32_bf16 v[92:95], v[28:31], v[180:183], v[92:95]\n"
    "ds_read_b64_tr_b16 v[24:25], v1 offset:61440\n"
    "v_mfma_f32_16x16x32_bf16 v[88:91], v[28:31], v[176:179], v[88:91]\n"
    "ds_read_b64_tr_b16 v[26:27], v1 offset:61952\n"
    "ds_read_b128 v[204:207], v5 offset:2048\n"
    "s_waitcnt lgkmcnt(7)\n"
    "v_mfma_f32_16x16x32_bf16 v[64:67], v[16:19], v[184:187], v[64:67]\n"
    "ds_read_b64_tr_b16 v[28:29], v2 offset:61440\n"
    "v_mfma_f32_16x16x32_bf16 v[68:71], v[16:19], v[188:191], v[68:71]\n"
    "ds_read_b64_tr_b16 v[30:31], v2 offset:61952\n"
    "s_waitcnt lgkmcnt(6)\n"
    "v_mfma_f32_16x16x32_bf16 v[76:79], v[20:23], v[188:191], v[76:79]\n"
    "v_add_u32 v1, 0x10000, v1\n"
    "ds_read_b64_tr_b16 v[16:17], v1 offset:0\n"
    "v_mfma_f32_16x16x32_bf16 v[72:75], v[20:23], v[184:187], v[72:75]\n"
    "ds_read_b64_tr_b16 v[18:19], v1 offset:512\n"
    "s_waitcnt lgkmcnt(5)\n"
    "v_mfma_f32_16x16x32_bf16 v[80:83], v[24:27], v[184:187], v[80:83]\n"
    "v_add_u32 v2, 0x10000, v2\n"
    "ds_read_b64_tr_b16 v[20:21], v2 offset:0\n"
    "v_mfma_f32_16x16x32_bf16 v[84:87], v[24:27], v[188:191], v[84:87]\n"
    "ds_read_b64_tr_b16 v[22:23], v2 offset:512\n"
    "s_waitcnt lgkmcnt(4)\n"
    "v_mfma_f32_16x16x32_bf16 v[92:95], v[28:31], v[188:191], v[92:95]\n"
    "ds_read_b64_tr_b16 v[24:25], v1 offset:8192\n"
    "v_mfma_f32_16x16x32_bf16 v[88:91], v[28:31], v[184:187], v[88:91]\n"
    "ds_read_b64_tr_b16 v[26:27], v1 offset:8704\n"
    "s_waitcnt vmcnt(4)\n"
    "s_cbranch_vccz .Lfz_RET_bY9\n"
    "s_barrier\n"
    ".Lfz_RET_bY9:\n"
    "s_add_u32 m0, s52, 0x8000\n"
    "s_lshl_b32 s54, s63, 7\n"
    "s_add_u32 s54, s54, s86\n"
    "buffer_load_dwordx4 v32, s[44:47], s54 offen sc1 lds\n"
    "s_and_b32 s87, s48, 1\n"
    "s_lshl_b32 s87, s87, 1\n"
    "v_xor_b32 v39, s87, v39\n"
    "v_and_b32 v36, 1, v0\n"
    "s_add_u32 m0, s52, 0xa000\n"
    "s_lshl_b32 s55, s63, 7\n"
    "s_add_u32 s55, s55, s86\n"
    "s_add_u32 s55, s55, 0x10000\n"
    "buffer_load_dwordx4 v32, s[44:47], s55 offen sc1 lds\n"
    "v_lshl_or_b32 v39, v39, 1, v36\n"
    "v_lshl_or_b32 v39, v39, 3, v34\n"
    "v_and_b32 v36, 3, v0\n"
    "s_add_u32 m0, s52, 0x1c000\n"
    "s_mul_i32 s54, s63, 0x30000\n"
    "s_add_u32 s54, s54, 0x400\n"
    "buffer_load_dwordx4 v7, s[16:19], s54 offen lds\n"
    "v_xor_b32 v40, v36, v35\n"
    "v_lshl_or_b32 v40, v40, 4, v34\n"
    "s_and_b32 s87, s48, 2\n"
    "s_lshl_b32 s94, s87, 2\n"
    "s_lshl_b32 s87, s87, 12\n"
    "v_add_u32 v39, s87, v39\n"
    "v_add_u32 v40, s94, v40\n"
    "s_add_u32 m0, s52, 0x1e000\n"
    "s_mul_i32 s55, s63, 0x30000\n"
    "s_add_u32 s55, s55, 0x800\n"
    "buffer_load_dwordx4 v7, s[16:19], s55 offen lds\n"
    "s_cbranch_vccnz .Lfz_RET_bY10\n"
    "s_barrier\n"
    ".Lfz_RET_bY10:\n"
    "s_waitcnt lgkmcnt(4)\n"
    "v_mfma_f32_16x16x32_bf16 v[64:67], v[16:19], v[192:195], v[64:67]\n"
    "ds_read_b64_tr_b16 v[28:29], v2 offset:8192\n"
    "v_mfma_f32_16x16x32_bf16 v[68:71], v[16:19], v[196:199], v[68:71]\n"
    "ds_read_b64_tr_b16 v[30:31], v2 offset:8704\n"
    "v_add_u32 v4, 0xfffdc000, v4\n"
    "ds_read_b128 v[208:211], v4 offset:0\n"
    "s_waitcnt lgkmcnt(5)\n"
    "v_mfma_f32_16x16x32_bf16 v[76:79], v[20:23], v[196:199], v[76:79]\n"
    "ds_read_b64_tr_b16 v[16:17], v1 offset:4096\n"
    "v_mfma_f32_16x16x32_bf16 v[72:75], v[20:23], v[192:195], v[72:75]\n"
    "ds_read_b64_tr_b16 v[18:19], v1 offset:4608\n"
    "ds_read_b128 v[212:215], v4 offset:2048\n"
    "s_waitcnt lgkmcnt(6)\n"
    "v_mfma_f32_16x16x32_bf16 v[80:83], v[24:27], v[192:195], v[80:83]\n"
    "ds_read_b64_tr_b16 v[20:21], v2 offset:4096\n"
    "v_mfma_f32_16x16x32_bf16 v[84:87], v[24:27], v[196:199], v[84:87]\n"
    "ds_read_b64_tr_b16 v[22:23], v2 offset:4608\n"
    "v_add_u32 v5, 0xfffdc000, v5\n"
    "ds_read_b128 v[216:219], v5 offset:0\n"
    "s_waitcnt lgkmcnt(7)\n"
    "v_mfma_f32_16x16x32_bf16 v[92:95], v[28:31], v[196:199], v[92:95]\n"
    "ds_read_b64_tr_b16 v[24:25], v1 offset:12288\n"
    "v_mfma_f32_16x16x32_bf16 v[88:91], v[28:31], v[192:195], v[88:91]\n"
    "ds_read_b64_tr_b16 v[26:27], v1 offset:12800\n"
    "ds_read_b128 v[220:223], v5 offset:2048\n"
    "s_waitcnt lgkmcnt(7)\n"
    "v_mfma_f32_16x16x32_bf16 v[64:67], v[16:19], v[200:203], v[64:67]\n"
    "ds_read_b64_tr_b16 v[28:29], v2 offset:12288\n"
    "v_mfma_f32_16x16x32_bf16 v[68:71], v[16:19], v[204:207], v[68:71]\n"
    "ds_read_b64_tr_b16 v[30:31], v2 offset:12800\n"
    "s_waitcnt lgkmcnt(6)\n"
    "v_mfma_f32_16x16x32_bf16 v[76:79], v[20:23], v[204:207], v[76:79]\n"
    "ds_read_b64_tr_b16 v[16:17], v1 offset:16384\n"
    "v_mfma_f32_16x16x32_bf16 v[72:75], v[20:23], v[200:203], v[72:75]\n"
    "ds_read_b64_tr_b16 v[18:19], v1 offset:16896\n"
    "s_waitcnt lgkmcnt(5)\n"
    "v_mfma_f32_16x16x32_bf16 v[80:83], v[24:27], v[200:203], v[80:83]\n"
    "ds_read_b64_tr_b16 v[20:21], v2 offset:16384\n"
    "v_mfma_f32_16x16x32_bf16 v[84:87], v[24:27], v[204:207], v[84:87]\n"
    "ds_read_b64_tr_b16 v[22:23], v2 offset:16896\n"
    "s_waitcnt lgkmcnt(4)\n"
    "v_mfma_f32_16x16x32_bf16 v[92:95], v[28:31], v[204:207], v[92:95]\n"
    "ds_read_b64_tr_b16 v[24:25], v1 offset:24576\n"
    "v_mfma_f32_16x16x32_bf16 v[88:91], v[28:31], v[200:203], v[88:91]\n"
    "ds_read_b64_tr_b16 v[26:27], v1 offset:25088\n"
    "s_waitcnt vmcnt(4)\n"
    "s_cbranch_vccz .Lfz_RET_bY11\n"
    "s_barrier\n"
    ".Lfz_RET_bY11:\n"
    "s_add_u32 m0, s52, 0x20000\n"
    "s_mul_i32 s54, s70, 0x38000\n"
    "buffer_load_dwordx4 v8, s[28:31], s54 offen lds\n"
    "s_add_u32 m0, s52, 0x22000\n"
    "s_mul_i32 s55, s70, 0x38000\n"
    "buffer_load_dwordx4 v8, s[32:35], s55 offen lds\n"
    "s_cbranch_vccnz .Lfz_RET_bY12\n"
    "s_barrier\n"
    ".Lfz_RET_bY12:\n"
    "s_waitcnt lgkmcnt(4)\n"
    "v_mfma_f32_16x16x32_bf16 v[64:67], v[16:19], v[208:211], v[64:67]\n"
    "ds_read_b64_tr_b16 v[28:29], v2 offset:24576\n"
    "v_mfma_f32_16x16x32_bf16 v[68:71], v[16:19], v[212:215], v[68:71]\n"
    "ds_read_b64_tr_b16 v[30:31], v2 offset:25088\n"
    "ds_read_b128 v[224:227], v4 offset:16384\n"
    "s_waitcnt lgkmcnt(5)\n"
    "v_mfma_f32_16x16x32_bf16 v[76:79], v[20:23], v[212:215], v[76:79]\n"
    "ds_read_b64_tr_b16 v[16:17], v1 offset:20480\n"
    "v_mfma_f32_16x16x32_bf16 v[72:75], v[20:23], v[208:211], v[72:75]\n"
    "ds_read_b64_tr_b16 v[18:19], v1 offset:20992\n"
    "ds_read_b128 v[228:231], v4 offset:18432\n"
    "s_waitcnt lgkmcnt(6)\n"
    "v_mfma_f32_16x16x32_bf16 v[80:83], v[24:27], v[208:211], v[80:83]\n"
    "ds_read_b64_tr_b16 v[20:21], v2 offset:20480\n"
    "v_mfma_f32_16x16x32_bf16 v[84:87], v[24:27], v[212:215], v[84:87]\n"
    "ds_read_b64_tr_b16 v[22:23], v2 offset:20992\n"
    "ds_read_b128 v[232:235], v5 offset:16384\n"
    "s_waitcnt lgkmcnt(7)\n"
    "v_mfma_f32_16x16x32_bf16 v[92:95], v[28:31], v[212:215], v[92:95]\n"
    "ds_read_b64_tr_b16 v[24:25], v1 offset:28672\n"
    "v_mfma_f32_16x16x32_bf16 v[88:91], v[28:31], v[208:211], v[88:91]\n"
    "ds_read_b64_tr_b16 v[26:27], v1 offset:29184\n"
    "ds_read_b128 v[236:239], v5 offset:18432\n"
    "s_waitcnt lgkmcnt(7)\n"
    "v_mfma_f32_16x16x32_bf16 v[64:67], v[16:19], v[216:219], v[64:67]\n"
    "ds_read_b64_tr_b16 v[28:29], v2 offset:28672\n"
    "v_mfma_f32_16x16x32_bf16 v[68:71], v[16:19], v[220:223], v[68:71]\n"
    "ds_read_b64_tr_b16 v[30:31], v2 offset:29184\n"
    "s_waitcnt lgkmcnt(6)\n"
    "v_mfma_f32_16x16x32_bf16 v[76:79], v[20:23], v[220:223], v[76:79]\n"
    "ds_read_b64_tr_b16 v[16:17], v1 offset:32768\n"
    "v_mfma_f32_16x16x32_bf16 v[72:75], v[20:23], v[216:219], v[72:75]\n"
    "ds_read_b64_tr_b16 v[18:19], v1 offset:33280\n"
    "s_waitcnt lgkmcnt(5)\n"
    "v_mfma_f32_16x16x32_bf16 v[80:83], v[24:27], v[216:219], v[80:83]\n"
    "ds_read_b64_tr_b16 v[20:21], v2 offset:32768\n"
    "v_mfma_f32_16x16x32_bf16 v[84:87], v[24:27], v[220:223], v[84:87]\n"
    "ds_read_b64_tr_b16 v[22:23], v2 offset:33280\n"
    "s_waitcnt lgkmcnt(4)\n"
    "v_mfma_f32_16x16x32_bf16 v[92:95], v[28:31], v[220:223], v[92:95]\n"
    "ds_read_b64_tr_b16 v[24:25], v1 offset:40960\n"
    "v_mfma_f32_16x16x32_bf16 v[88:91], v[28:31], v[216:219], v[88:91]\n"
    "ds_read_b64_tr_b16 v[26:27], v1 offset:41472\n"
    "s_waitcnt vmcnt(2)\n"
    "s_cbranch_vccz .Lfz_RET_bY13\n"
    "s_barrier\n"
    ".Lfz_RET_bY13:\n"
    "s_add_u32 m0, s52, 0x24000\n"
    "s_mul_i32 s54, s71, 0x38000\n"
    "buffer_load_dwordx4 v8, s[28:31], s54 offen lds\n"
    "v_lshl_or_b32 v7, v39, 16, v37\n"
    "s_add_u32 m0, s52, 0x26000\n"
    "s_mul_i32 s55, s71, 0x38000\n"
    "buffer_load_dwordx4 v8, s[32:35], s55 offen lds\n"
    "v_lshl_or_b32 v14, v40, 16, v38\n"
    "s_cbranch_vccnz .Lfz_RET_bY14\n"
    "s_barrier\n"
    ".Lfz_RET_bY14:\n"
    "s_waitcnt lgkmcnt(4)\n"
    "v_mfma_f32_16x16x32_bf16 v[64:67], v[16:19], v[224:227], v[64:67]\n"
    "ds_read_b64_tr_b16 v[28:29], v2 offset:40960\n"
    "v_mfma_f32_16x16x32_bf16 v[68:71], v[16:19], v[228:231], v[68:71]\n"
    "ds_read_b64_tr_b16 v[30:31], v2 offset:41472\n"
    "ds_read_b128 v[240:243], v4 offset:32768\n"
    "s_waitcnt lgkmcnt(5)\n"
    "v_mfma_f32_16x16x32_bf16 v[76:79], v[20:23], v[228:231], v[76:79]\n"
    "ds_read_b64_tr_b16 v[16:17], v1 offset:36864\n"
    "v_mfma_f32_16x16x32_bf16 v[72:75], v[20:23], v[224:227], v[72:75]\n"
    "ds_read_b64_tr_b16 v[18:19], v1 offset:37376\n"
    "ds_read_b128 v[244:247], v4 offset:34816\n"
    "s_waitcnt lgkmcnt(6)\n"
    "v_mfma_f32_16x16x32_bf16 v[80:83], v[24:27], v[224:227], v[80:83]\n"
    "ds_read_b64_tr_b16 v[20:21], v2 offset:36864\n"
    "v_mfma_f32_16x16x32_bf16 v[84:87], v[24:27], v[228:231], v[84:87]\n"
    "ds_read_b64_tr_b16 v[22:23], v2 offset:37376\n"
    "ds_read_b128 v[248:251], v5 offset:32768\n"
    "s_waitcnt lgkmcnt(7)\n"
    "v_mfma_f32_16x16x32_bf16 v[92:95], v[28:31], v[228:231], v[92:95]\n"
    "ds_read_b64_tr_b16 v[24:25], v1 offset:45056\n"
    "v_mfma_f32_16x16x32_bf16 v[88:91], v[28:31], v[224:227], v[88:91]\n"
    "ds_read_b64_tr_b16 v[26:27], v1 offset:45568\n"
    "ds_read_b128 v[252:255], v5 offset:34816\n"
    "s_waitcnt lgkmcnt(7)\n"
    "v_mfma_f32_16x16x32_bf16 v[64:67], v[16:19], v[232:235], v[64:67]\n"
    "ds_read_b64_tr_b16 v[28:29], v2 offset:45056\n"
    "v_mfma_f32_16x16x32_bf16 v[68:71], v[16:19], v[236:239], v[68:71]\n"
    "ds_read_b64_tr_b16 v[30:31], v2 offset:45568\n"
    "s_waitcnt lgkmcnt(6)\n"
    "v_mfma_f32_16x16x32_bf16 v[76:79], v[20:23], v[236:239], v[76:79]\n"
    "ds_read_b64_tr_b16 v[16:17], v1 offset:49152\n"
    "v_mfma_f32_16x16x32_bf16 v[72:75], v[20:23], v[232:235], v[72:75]\n"
    "ds_read_b64_tr_b16 v[18:19], v1 offset:49664\n"
    "s_waitcnt lgkmcnt(5)\n"
    "v_mfma_f32_16x16x32_bf16 v[80:83], v[24:27], v[232:235], v[80:83]\n"
    "ds_read_b64_tr_b16 v[20:21], v2 offset:49152\n"
    "v_mfma_f32_16x16x32_bf16 v[84:87], v[24:27], v[236:239], v[84:87]\n"
    "ds_read_b64_tr_b16 v[22:23], v2 offset:49664\n"
    "s_waitcnt lgkmcnt(4)\n"
    "v_mfma_f32_16x16x32_bf16 v[92:95], v[28:31], v[236:239], v[92:95]\n"
    "ds_read_b64_tr_b16 v[24:25], v1 offset:57344\n"
    "v_mfma_f32_16x16x32_bf16 v[88:91], v[28:31], v[232:235], v[88:91]\n"
    "ds_read_b64_tr_b16 v[26:27], v1 offset:57856\n"
    "s_waitcnt vmcnt(2)\n"
    "s_cbranch_vccz .Lfz_RET_bY15\n"
    "s_barrier\n"
    ".Lfz_RET_bY15:\n"
    "s_add_u32 m0, s52, 0x0\n"
    "s_mul_i32 s54, s72, 0x38000\n"
    "buffer_load_dwordx4 v8, s[28:31], s54 offen lds\n"
    "s_add_u32 m0, s52, 0x2000\n"
    "s_mul_i32 s55, s72, 0x38000\n"
    "buffer_load_dwordx4 v8, s[32:35], s55 offen lds\n"
    "s_cbranch_vccnz .Lfz_RET_bY16\n"
    "s_barrier\n"
    ".Lfz_RET_bY16:\n"
    "s_waitcnt lgkmcnt(4)\n"
    "v_mfma_f32_16x16x32_bf16 v[64:67], v[16:19], v[240:243], v[64:67]\n"
    "ds_read_b64_tr_b16 v[28:29], v2 offset:57344\n"
    "v_mfma_f32_16x16x32_bf16 v[68:71], v[16:19], v[244:247], v[68:71]\n"
    "ds_read_b64_tr_b16 v[30:31], v2 offset:57856\n"
    "s_waitcnt lgkmcnt(4)\n"
    "v_mfma_f32_16x16x32_bf16 v[76:79], v[20:23], v[244:247], v[76:79]\n"
    "ds_read_b64_tr_b16 v[16:17], v1 offset:53248\n"
    "v_mfma_f32_16x16x32_bf16 v[72:75], v[20:23], v[240:243], v[72:75]\n"
    "ds_read_b64_tr_b16 v[18:19], v1 offset:53760\n"
    "s_waitcnt lgkmcnt(4)\n"
    "v_mfma_f32_16x16x32_bf16 v[80:83], v[24:27], v[240:243], v[80:83]\n"
    "ds_read_b64_tr_b16 v[20:21], v2 offset:53248\n"
    "v_mfma_f32_16x16x32_bf16 v[84:87], v[24:27], v[244:247], v[84:87]\n"
    "ds_read_b64_tr_b16 v[22:23], v2 offset:53760\n"
    "s_waitcnt lgkmcnt(4)\n"
    "v_mfma_f32_16x16x32_bf16 v[92:95], v[28:31], v[244:247], v[92:95]\n"
    "ds_read_b64_tr_b16 v[24:25], v1 offset:61440\n"
    "v_mfma_f32_16x16x32_bf16 v[88:91], v[28:31], v[240:243], v[88:91]\n"
    "ds_read_b64_tr_b16 v[26:27], v1 offset:61952\n"
    "s_waitcnt lgkmcnt(4)\n"
    "v_mfma_f32_16x16x32_bf16 v[64:67], v[16:19], v[248:251], v[64:67]\n"
    "ds_read_b64_tr_b16 v[28:29], v2 offset:61440\n"
    "v_mfma_f32_16x16x32_bf16 v[68:71], v[16:19], v[252:255], v[68:71]\n"
    "ds_read_b64_tr_b16 v[30:31], v2 offset:61952\n"
    "s_waitcnt lgkmcnt(4)\n"
    "v_mfma_f32_16x16x32_bf16 v[76:79], v[20:23], v[252:255], v[76:79]\n"
    "v_add_u32 v3, 0x20000, v3\n"
    "ds_read_b64_tr_b16 v[16:17], v3 offset:0\n"
    "v_mfma_f32_16x16x32_bf16 v[72:75], v[20:23], v[248:251], v[72:75]\n"
    "ds_read_b64_tr_b16 v[18:19], v3 offset:256\n"
    "s_waitcnt lgkmcnt(4)\n"
    "v_mfma_f32_16x16x32_bf16 v[80:83], v[24:27], v[248:251], v[80:83]\n"
    "ds_read_b64_tr_b16 v[20:21], v3 offset:8192\n"
    "v_mfma_f32_16x16x32_bf16 v[84:87], v[24:27], v[252:255], v[84:87]\n"
    "ds_read_b64_tr_b16 v[22:23], v3 offset:8448\n"
    "s_waitcnt lgkmcnt(4)\n"
    "v_mfma_f32_16x16x32_bf16 v[92:95], v[28:31], v[252:255], v[92:95]\n"
    "ds_read_b64_tr_b16 v[24:25], v3 offset:2048\n"
    "v_mfma_f32_16x16x32_bf16 v[88:91], v[28:31], v[248:251], v[88:91]\n"
    "ds_read_b64_tr_b16 v[26:27], v3 offset:2304\n"
    "s_waitcnt vmcnt(2)\n"
    "s_cbranch_vccz .Lfz_RET_bY17\n"
    "s_barrier\n"
    ".Lfz_RET_bY17:\n"
    "s_add_u32 m0, s52, 0x4000\n"
    "s_mul_i32 s54, s73, 0x38000\n"
    "buffer_load_dwordx4 v8, s[28:31], s54 offen lds\n"
    "v_cvt_pk_bf16_f32 v32, v64, v65\n"
    "v_cvt_pk_bf16_f32 v33, v66, v67\n"
    "v_cvt_pk_bf16_f32 v34, v72, v73\n"
    "v_cvt_pk_bf16_f32 v35, v74, v75\n"
    "v_cvt_pk_bf16_f32 v36, v68, v69\n"
    "v_cvt_pk_bf16_f32 v37, v70, v71\n"
    "v_cvt_pk_bf16_f32 v38, v76, v77\n"
    "v_cvt_pk_bf16_f32 v39, v78, v79\n"
    "v_cvt_pk_bf16_f32 v40, v80, v81\n"
    "v_cvt_pk_bf16_f32 v41, v82, v83\n"
    "v_cvt_pk_bf16_f32 v42, v88, v89\n"
    "v_cvt_pk_bf16_f32 v43, v90, v91\n"
    "v_cvt_pk_bf16_f32 v44, v84, v85\n"
    "v_cvt_pk_bf16_f32 v45, v86, v87\n"
    "v_cvt_pk_bf16_f32 v46, v92, v93\n"
    "v_cvt_pk_bf16_f32 v47, v94, v95\n"
    "v_mfma_f32_16x16x32_bf16 v[64:67], v[48:51], v[32:35], 0\n"
    "v_mfma_f32_16x16x32_bf16 v[68:71], v[52:55], v[32:35], 0\n"
    "v_mfma_f32_16x16x32_bf16 v[72:75], v[56:59], v[32:35], 0\n"
    "v_mfma_f32_16x16x32_bf16 v[76:79], v[60:63], v[32:35], 0\n"
    "v_mfma_f32_16x16x32_bf16 v[80:83], v[48:51], v[36:39], 0\n"
    "v_mfma_f32_16x16x32_bf16 v[84:87], v[52:55], v[36:39], 0\n"
    "v_mfma_f32_16x16x32_bf16 v[88:91], v[56:59], v[36:39], 0\n"
    "v_mfma_f32_16x16x32_bf16 v[92:95], v[60:63], v[36:39], 0\n"
    "s_nop 0\n"
    "v_max3_f32 v48, v64, v65, v66\n"
    "v_max3_f32 v49, v67, v68, v69\n"
    "v_max3_f32 v48, v48, v70, v71\n"
    "v_max3_f32 v49, v49, v72, v73\n"
    "v_max3_f32 v48, v48, v74, v75\n"
    "v_max3_f32 v49, v49, v76, v77\n"
    "v_max3_f32 v48, v48, v78, v79\n"
    "v_max_f32 v48, v48, v49\n"
    "v_mov_b32 v50, 0x3fb8aa3b\n"
    "v_mul_f32 v48, 0xbfb8aa3b, v48\n"
    "v_pk_fma_f32 v[64:65], v[64:65], v[50:51], v[48:49] op_sel_hi:[1,0,0]\n"
    "v_pk_fma_f32 v[66:67], v[66:67], v[50:51], v[48:49] op_sel_hi:[1,0,0]\n"
    "v_pk_fma_f32 v[68:69], v[68:69], v[50:51], v[48:49] op_sel_hi:[1,0,0]\n"
    "v_pk_fma_f32 v[70:71], v[70:71], v[50:51], v[48:49] op_sel_hi:[1,0,0]\n"
    "v_pk_fma_f32 v[72:73], v[72:73], v[50:51], v[48:49] op_sel_hi:[1,0,0]\n"
    "v_pk_fma_f32 v[74:75], v[74:75], v[50:51], v[48:49] op_sel_hi:[1,0,0]\n"
    "v_pk_fma_f32 v[76:77], v[76:77], v[50:51], v[48:49] op_sel_hi:[1,0,0]\n"
    "v_pk_fma_f32 v[78:79], v[78:79], v[50:51], v[48:49] op_sel_hi:[1,0,0]\n"
    "v_exp_f32 v64, v64\n"
    "v_exp_f32 v65, v65\n"
    "v_exp_f32 v66, v66\n"
    "v_exp_f32 v67, v67\n"
    "v_exp_f32 v68, v68\n"
    "v_exp_f32 v69, v69\n"
    "v_pk_add_f32 v[48:49], v[64:65], v[66:67]\n"
    "v_exp_f32 v70, v70\n"
    "v_exp_f32 v71, v71\n"
    "v_pk_add_f32 v[48:49], v[48:49], v[68:69]\n"
    "v_exp_f32 v72, v72\n"
    "v_exp_f32 v73, v73\n"
    "v_pk_add_f32 v[48:49], v[48:49], v[70:71]\n"
    "v_exp_f32 v74, v74\n"
    "v_exp_f32 v75, v75\n"
    "v_pk_add_f32 v[48:49], v[48:49], v[72:73]\n"
    "v_exp_f32 v76, v76\n"
    "v_exp_f32 v77, v77\n"
    "v_pk_add_f32 v[48:49], v[48:49], v[74:75]\n"
    "v_exp_f32 v78, v78\n"
    "v_exp_f32 v79, v79\n"
    "v_pk_add_f32 v[48:49], v[48:49], v[76:77]\n"
    "v_pk_add_f32 v[48:49], v[48:49], v[78:79]\n"
    "v_add_f32 v48, v48, v49\n"
    "v_rcp_f32 v48, v48\n"
    "s_nop 0\n"
    "v_pk_mul_f32 v[64:65], v[64:65], v[48:49] op_sel_hi:[1,0]\n"
    "v_pk_mul_f32 v[66:67], v[66:67], v[48:49] op_sel_hi:[1,0]\n"
    "v_pk_mul_f32 v[68:69], v[68:69], v[48:49] op_sel_hi:[1,0]\n"
    "v_pk_mul_f32 v[70:71], v[70:71], v[48:49] op_sel_hi:[1,0]\n"
    "v_pk_mul_f32 v[72:73], v[72:73], v[48:49] op_sel_hi:[1,0]\n"
    "s_add_u32 m0, s52, 0x6000\n"
    "s_mul_i32 s55, s73, 0x38000\n"
    "buffer_load_dwordx4 v8, s[32:35], s55 offen lds\n"
    "v_pk_mul_f32 v[74:75], v[74:75], v[48:49] op_sel_hi:[1,0]\n"
    "v_pk_mul_f32 v[76:77], v[76:77], v[48:49] op_sel_hi:[1,0]\n"
    "v_pk_mul_f32 v[78:79], v[78:79], v[48:49] op_sel_hi:[1,0]\n"
    "v_cvt_pk_bf16_f32 v32, v64, v65\n"
    "v_cvt_pk_bf16_f32 v33, v66, v67\n"
    "v_cvt_pk_bf16_f32 v34, v68, v69\n"
    "v_cvt_pk_bf16_f32 v35, v70, v71\n"
    "v_cvt_pk_bf16_f32 v36, v72, v73\n"
    "v_cvt_pk_bf16_f32 v37, v74, v75\n"
    "v_cvt_pk_bf16_f32 v38, v76, v77\n"
    "v_cvt_pk_bf16_f32 v39, v78, v79\n"
    "v_max3_f32 v48, v80, v81, v82\n"
    "v_max3_f32 v49, v83, v84, v85\n"
    "v_max3_f32 v48, v48, v86, v87\n"
    "v_max3_f32 v49, v49, v88, v89\n"
    "v_max3_f32 v48, v48, v90, v91\n"
    "v_max3_f32 v49, v49, v92, v93\n"
    "v_max3_f32 v48, v48, v94, v95\n"
    "v_max_f32 v48, v48, v49\n"
    "v_mov_b32 v50, 0x3fb8aa3b\n"
    "v_mul_f32 v48, 0xbfb8aa3b, v48\n"
    "v_pk_fma_f32 v[80:81], v[80:81], v[50:51], v[48:49] op_sel_hi:[1,0,0]\n"
    "v_pk_fma_f32 v[82:83], v[82:83], v[50:51], v[48:49] op_sel_hi:[1,0,0]\n"
    "v_pk_fma_f32 v[84:85], v[84:85], v[50:51], v[48:49] op_sel_hi:[1,0,0]\n"
    "v_pk_fma_f32 v[86:87], v[86:87], v[50:51], v[48:49] op_sel_hi:[1,0,0]\n"
    "v_pk_fma_f32 v[88:89], v[88:89], v[50:51], v[48:49] op_sel_hi:[1,0,0]\n"
    "v_pk_fma_f32 v[90:91], v[90:91], v[50:51], v[48:49] op_sel_hi:[1,0,0]\n"
    "v_pk_fma_f32 v[92:93], v[92:93], v[50:51], v[48:49] op_sel_hi:[1,0,0]\n"
    "v_pk_fma_f32 v[94:95], v[94:95], v[50:51], v[48:49] op_sel_hi:[1,0,0]\n"
    "v_exp_f32 v80, v80\n"
    "v_exp_f32 v81, v81\n"
    "v_exp_f32 v82, v82\n"
    "v_exp_f32 v83, v83\n"
    "v_exp_f32 v84, v84\n"
    "v_exp_f32 v85, v85\n"
    "v_pk_add_f32 v[48:49], v[80:81], v[82:83]\n"
    "v_exp_f32 v86, v86\n"
    "v_exp_f32 v87, v87\n"
    "v_pk_add_f32 v[48:49], v[48:49], v[84:85]\n"
    "v_exp_f32 v88, v88\n"
    "v_exp_f32 v89, v89\n"
    "v_pk_add_f32 v[48:49], v[48:49], v[86:87]\n"
    "v_exp_f32 v90, v90\n"
    "v_exp_f32 v91, v91\n"
    "v_pk_add_f32 v[48:49], v[48:49], v[88:89]\n"
    "v_exp_f32 v92, v92\n"
    "v_exp_f32 v93, v93\n"
    "v_pk_add_f32 v[48:49], v[48:49], v[90:91]\n"
    "v_exp_f32 v94, v94\n"
    "v_exp_f32 v95, v95\n"
    "v_pk_add_f32 v[48:49], v[48:49], v[92:93]\n"
    "v_pk_add_f32 v[48:49], v[48:49], v[94:95]\n"
    "v_add_f32 v48, v48, v49\n"
    "v_rcp_f32 v48, v48\n"
    "s_nop 0\n"
    "v_pk_mul_f32 v[80:81], v[80:81], v[48:49] op_sel_hi:[1,0]\n"
    "v_pk_mul_f32 v[82:83], v[82:83], v[48:49] op_sel_hi:[1,0]\n"
    "v_pk_mul_f32 v[84:85], v[84:85], v[48:49] op_sel_hi:[1,0]\n"
    "v_pk_mul_f32 v[86:87], v[86:87], v[48:49] op_sel_hi:[1,0]\n"
    "v_pk_mul_f32 v[88:89], v[88:89], v[48:49] op_sel_hi:[1,0]\n"
    "v_pk_mul_f32 v[90:91], v[90:91], v[48:49] op_sel_hi:[1,0]\n"
    "v_pk_mul_f32 v[92:93], v[92:93], v[48:49] op_sel_hi:[1,0]\n"
    "v_pk_mul_f32 v[94:95], v[94:95], v[48:49] op_sel_hi:[1,0]\n"
    "v_cvt_pk_bf16_f32 v56, v80, v81\n"
    "v_cvt_pk_bf16_f32 v57, v82, v83\n"
    "v_cvt_pk_bf16_f32 v58, v84, v85\n"
    "v_cvt_pk_bf16_f32 v59, v86, v87\n"
    "v_cvt_pk_bf16_f32 v60, v88, v89\n"
    "v_cvt_pk_bf16_f32 v61, v90, v91\n"
    "v_cvt_pk_bf16_f32 v62, v92, v93\n"
    "v_cvt_pk_bf16_f32 v63, v94, v95\n"
    "s_cmp_eq_u32 s84, 0\n"
    "s_mov_b32 s87, 0x18000\n"
    "s_cselect_b32 s87, s87, 0x14000\n"
    "s_lshl_b32 s94, s84, 13\n"
    "s_add_u32 s94, s94, 0x10000\n"
    "v_add_u32_sdwa v48, s87, v7 dst_sel:DWORD dst_unused:UNUSED_PAD src0_sel:DWORD src1_sel:WORD_0\n"
    "v_add_u32_sdwa v50, s94, v14 dst_sel:DWORD dst_unused:UNUSED_PAD src0_sel:DWORD src1_sel:WORD_0\n"
    "v_add_u32_sdwa v52, s87, v7 dst_sel:DWORD dst_unused:UNUSED_PAD src0_sel:DWORD src1_sel:WORD_1\n"
    "v_add_u32_sdwa v54, s94, v14 dst_sel:DWORD dst_unused:UNUSED_PAD src0_sel:DWORD src1_sel:WORD_1\n"
    "v_xor_b32 v49, 16, v48\n"
    "v_xor_b32 v53, 32, v52\n"
    "v_xor_b32 v55, 32, v54\n"
    "s_cbranch_vccnz .Lfz_RET_bY18\n"
    "s_barrier\n"
    ".Lfz_RET_bY18:\n"
    "s_waitcnt lgkmcnt(4)\n"
    "v_mfma_f32_16x16x32_bf16 v[96:99], v[16:19], v[128:131], 0\n"
    "ds_read_b64_tr_b16 v[28:29], v3 offset:10240\n"
    "v_mfma_f32_16x16x32_bf16 v[100:103], v[16:19], v[132:135], 0\n"
    "ds_read_b64_tr_b16 v[30:31], v3 offset:10496\n"
    "ds_write_b128 v48, v[32:35] offset:0\n"
    "s_waitcnt lgkmcnt(5)\n"
    "v_mfma_f32_16x16x32_bf16 v[108:111], v[20:23], v[132:135], 0\n"
    "ds_read_b64_tr_b16 v[16:17], v3 offset:4096\n"
    "v_mfma_f32_16x16x32_bf16 v[104:107], v[20:23], v[128:131], 0\n"
    "ds_read_b64_tr_b16 v[18:19], v3 offset:4352\n"
    "ds_write_b128 v49, v[36:39] offset:0\n"
    "s_waitcnt lgkmcnt(6)\n"
    "v_mfma_f32_16x16x32_bf16 v[96:99], v[24:27], v[136:139], v[96:99]\n"
    "ds_read_b64_tr_b16 v[20:21], v3 offset:12288\n"
    "v_mfma_f32_16x16x32_bf16 v[100:103], v[24:27], v[140:143], v[100:103]\n"
    "ds_read_b64_tr_b16 v[22:23], v3 offset:12544\n"
    "ds_write_b128 v50, v[40:43] offset:0\n"
    "s_waitcnt lgkmcnt(7)\n"
    "v_mfma_f32_16x16x32_bf16 v[108:111], v[28:31], v[140:143], v[108:111]\n"
    "ds_read_b64_tr_b16 v[24:25], v3 offset:6144\n"
    "v_mfma_f32_16x16x32_bf16 v[104:107], v[28:31], v[136:139], v[104:107]\n"
    "ds_read_b64_tr_b16 v[26:27], v3 offset:6400\n"
    "ds_write_b128 v48, v[56:59] offset:1024\n"
    "s_waitcnt lgkmcnt(7)\n"
    "v_mfma_f32_16x16x32_bf16 v[96:99], v[16:19], v[144:147], v[96:99]\n"
    "ds_read_b64_tr_b16 v[28:29], v3 offset:14336\n"
    "v_mfma_f32_16x16x32_bf16 v[100:103], v[16:19], v[148:151], v[100:103]\n"
    "ds_read_b64_tr_b16 v[30:31], v3 offset:14592\n"
    "ds_write_b128 v49, v[60:63] offset:1024\n"
    "s_waitcnt lgkmcnt(7)\n"
    "v_mfma_f32_16x16x32_bf16 v[108:111], v[20:23], v[148:151], v[108:111]\n"
    "ds_read_b64_tr_b16 v[16:17], v3 offset:16384\n"
    "v_mfma_f32_16x16x32_bf16 v[104:107], v[20:23], v[144:147], v[104:107]\n"
    "ds_read_b64_tr_b16 v[18:19], v3 offset:16640\n"
    "ds_write_b128 v50, v[44:47] offset:1024\n"
    "s_waitcnt lgkmcnt(7)\n"
    "v_mfma_f32_16x16x32_bf16 v[96:99], v[24:27], v[152:155], v[96:99]\n"
    "ds_read_b64_tr_b16 v[20:21], v3 offset:24576\n"
    "v_mfma_f32_16x16x32_bf16 v[100:103], v[24:27], v[156:159], v[100:103]\n"
    "ds_read_b64_tr_b16 v[22:23], v3 offset:24832\n"
    "s_waitcnt lgkmcnt(6)\n"
    "v_mfma_f32_16x16x32_bf16 v[108:111], v[28:31], v[156:159], v[108:111]\n"
    "ds_read_b64_tr_b16 v[24:25], v3 offset:18432\n"
    "v_mfma_f32_16x16x32_bf16 v[104:107], v[28:31], v[152:155], v[104:107]\n"
    "ds_read_b64_tr_b16 v[26:27], v3 offset:18688\n"
    "s_waitcnt vmcnt(2) lgkmcnt(4)\n"
    "s_cbranch_vccz .Lfz_RET_bY19\n"
    "s_barrier\n"
    ".Lfz_RET_bY19:\n"
    "s_add_u32 m0, s52, 0x8000\n"
    "s_mul_i32 s54, s56, 0x38000\n"
    "s_add_u32 s54, s54, 0x800\n"
    "buffer_load_dwordx4 v6, s[28:31], s54 offen lds\n"
    "v_lshrrev_b32 v4, 5, v0\n"
    "v_mul_u32_u24 v4, 18432, v4\n"
    "v_bfe_u32 v51, v0, 4, 1\n"
    "s_add_u32 m0, s52, 0xa000\n"
    "s_mul_i32 s55, s56, 0x38000\n"
    "s_add_u32 s55, s55, 0x800\n"
    "buffer_load_dwordx4 v6, s[32:35], s55 offen lds\n"
    "v_lshl_add_u32 v5, v51, 5, v4\n"
    "v_lshl_add_u32 v4, v51, 4, v4\n"
    "v_and_b32 v51, 15, v0\n"
    "s_add_u32 m0, s52, 0xc000\n"
    "s_mul_i32 s54, s57, 0x38000\n"
    "s_add_u32 s54, s54, 0x800\n"
    "buffer_load_dwordx4 v6, s[28:31], s54 offen lds\n"
    "v_lshl_add_u32 v5, v51, 4, v5\n"
    "v_lshl_add_u32 v4, v51, 5, v4\n"
    "v_mov_b32 v48, s99\n"
    "s_add_u32 m0, s52, 0xe000\n"
    "s_mul_i32 s55, s57, 0x38000\n"
    "s_add_u32 s55, s55, 0x800\n"
    "buffer_load_dwordx4 v6, s[32:35], s55 offen lds\n"
    "v_mov_b32 v49, s99\n"
    "v_mov_b32 v50, s99\n"
    "v_mov_b32 v51, s99\n"
    "s_cbranch_vccnz .Lfz_RET_bY20\n"
    "s_barrier\n"
    ".Lfz_RET_bY20:\n"
    "v_mfma_f32_16x16x32_bf16 v[96:99], v[16:19], v[160:163], v[96:99]\n"
    "ds_read_b64_tr_b16 v[28:29], v3 offset:26624\n"
    "v_mfma_f32_16x16x32_bf16 v[100:103], v[16:19], v[164:167], v[100:103]\n"
    "ds_read_b64_tr_b16 v[30:31], v3 offset:26880\n"
    "ds_read_b64_tr_b16 v[80:81], v52 offset:0\n"
    "ds_read_b64_tr_b16 v[82:83], v53 offset:256\n"
    "s_waitcnt lgkmcnt(6)\n"
    "v_mfma_f32_16x16x32_bf16 v[108:111], v[20:23], v[164:167], v[108:111]\n"
    "ds_read_b64_tr_b16 v[16:17], v3 offset:20480\n"
    "v_mfma_f32_16x16x32_bf16 v[104:107], v[20:23], v[160:163], v[104:107]\n"
    "ds_read_b64_tr_b16 v[18:19], v3 offset:20736\n"
    "ds_read_b64_tr_b16 v[32:33], v54 offset:0\n"
    "ds_read_b64_tr_b16 v[34:35], v55 offset:256\n"
    "s_waitcnt lgkmcnt(8)\n"
    "v_mfma_f32_16x16x32_bf16 v[96:99], v[24:27], v[168:171], v[96:99]\n"
    "ds_read_b64_tr_b16 v[20:21], v3 offset:28672\n"
    "v_mfma_f32_16x16x32_bf16 v[100:103], v[24:27], v[172:175], v[100:103]\n"
    "ds_read_b64_tr_b16 v[22:23], v3 offset:28928\n"
    "ds_read_b64_tr_b16 v[84:85], v52 offset:2048\n"
    "ds_read_b64_tr_b16 v[86:87], v53 offset:2304\n"
    "s_waitcnt lgkmcnt(10)\n"
    "v_mfma_f32_16x16x32_bf16 v[108:111], v[28:31], v[172:175], v[108:111]\n"
    "ds_read_b64_tr_b16 v[24:25], v3 offset:22528\n"
    "v_mfma_f32_16x16x32_bf16 v[104:107], v[28:31], v[168:171], v[104:107]\n"
    "ds_read_b64_tr_b16 v[26:27], v3 offset:22784\n"
    "ds_read_b64_tr_b16 v[36:37], v54 offset:2048\n"
    "ds_read_b64_tr_b16 v[38:39], v55 offset:2304\n"
    "s_waitcnt lgkmcnt(10)\n"
    "v_mfma_f32_16x16x32_bf16 v[96:99], v[16:19], v[176:179], v[96:99]\n"
    "ds_read_b64_tr_b16 v[28:29], v3 offset:30720\n"
    "v_mfma_f32_16x16x32_bf16 v[100:103], v[16:19], v[180:183], v[100:103]\n"
    "ds_read_b64_tr_b16 v[30:31], v3 offset:30976\n"
    "ds_read_b64_tr_b16 v[88:89], v52 offset:4096\n"
    "ds_read_b64_tr_b16 v[90:91], v53 offset:4352\n"
    "s_waitcnt lgkmcnt(10)\n"
    "v_mfma_f32_16x16x32_bf16 v[108:111], v[20:23], v[180:183], v[108:111]\n"
    "v_add_u32 v3, 0xfffe0000, v3\n"
    "ds_read_b64_tr_b16 v[16:17], v3 offset:0\n"
    "v_mfma_f32_16x16x32_bf16 v[104:107], v[20:23], v[176:179], v[104:107]\n"
    "ds_read_b64_tr_b16 v[18:19], v3 offset:256\n"
    "ds_read_b64_tr_b16 v[40:41], v54 offset:4096\n"
    "ds_read_b64_tr_b16 v[42:43], v55 offset:4352\n"
    "s_waitcnt lgkmcnt(10)\n"
    "v_mfma_f32_16x16x32_bf16 v[96:99], v[24:27], v[184:187], v[96:99]\n"
    "ds_read_b64_tr_b16 v[20:21], v3 offset:8192\n"
    "v_mfma_f32_16x16x32_bf16 v[100:103], v[24:27], v[188:191], v[100:103]\n"
    "ds_read_b64_tr_b16 v[22:23], v3 offset:8448\n"
    "ds_read_b64_tr_b16 v[92:93], v52 offset:6144\n"
    "ds_read_b64_tr_b16 v[94:95], v53 offset:6400\n"
    "s_waitcnt lgkmcnt(10)\n"
    "v_mfma_f32_16x16x32_bf16 v[108:111], v[28:31], v[188:191], v[108:111]\n"
    "ds_read_b64_tr_b16 v[24:25], v3 offset:2048\n"
    "v_mfma_f32_16x16x32_bf16 v[104:107], v[28:31], v[184:187], v[104:107]\n"
    "ds_read_b64_tr_b16 v[26:27], v3 offset:2304\n"
    "ds_read_b64_tr_b16 v[44:45], v54 offset:6144\n"
    "ds_read_b64_tr_b16 v[46:47], v55 offset:6400\n"
    "v_mfma_f32_16x16x32_bf16 v[56:59], v[80:83], v[32:35], 0\n"
    "v_mfma_f32_16x16x32_bf16 v[60:63], v[80:83], v[48:51], 0\n"
    "v_mfma_f32_16x16x32_bf16 v[56:59], v[84:87], v[36:39], v[56:59]\n"
    "v_mfma_f32_16x16x32_bf16 v[60:63], v[84:87], v[48:51], v[60:63]\n"
    "s_waitcnt lgkmcnt(8)\n"
    "v_mfma_f32_16x16x32_bf16 v[56:59], v[88:91], v[40:43], v[56:59]\n"
    "v_mfma_f32_16x16x32_bf16 v[60:63], v[88:91], v[48:51], v[60:63]\n"
    "s_waitcnt lgkmcnt(0)\n"
    "v_mfma_f32_16x16x32_bf16 v[56:59], v[92:95], v[44:47], v[56:59]\n"
    "v_mfma_f32_16x16x32_bf16 v[60:63], v[92:95], v[48:51], v[60:63]\n"
    "s_waitcnt vmcnt(4)\n"
    "s_cbranch_vccz .Lfz_RET_bY21\n"
    "s_barrier\n"
    ".Lfz_RET_bY21:\n"
    "s_add_u32 m0, s52, 0x1c000\n"
    "s_mul_i32 s54, s58, 0x38000\n"
    "s_add_u32 s54, s54, 0x800\n"
    "buffer_load_dwordx4 v6, s[28:31], s54 offen lds\n"
    "v_mov_b32 v33, s53\n"
    "v_mov_b32 v35, s53\n"
    "v_mov_b32 v37, s53\n"
    "v_mov_b32 v39, s53\n"
    "v_mov_b32 v41, s53\n"
    "v_mov_b32 v43, s53\n"
    "s_mov_b32 s90, 0xaaaaaaaa\n"
    "s_mov_b32 s91, 0xaaaaaaaa\n"
    "v_mov_b32 v32, v56\n"
    "v_mov_b32 v34, v57\n"
    "v_mov_b32 v36, v58\n"
    "v_mov_b32 v38, v59\n"
    "v_cndmask_b32 v40, v60, v62, s[90:91]\n"
    "v_cndmask_b32 v42, v61, v63, s[90:91]\n"
    "buffer_store_dwordx4 v[32:35], v4, s[44:47], s74 offen sc1\n"
    "buffer_store_dwordx4 v[36:39], v4, s[44:47], s74 offen offset:512 sc1\n"
    "s_mov_b32 exec_lo, 0x30003\n"
    "s_mov_b32 exec_hi, 0x30003\n"
    "buffer_store_dwordx4 v[40:43], v5, s[44:47], s74 offen offset:1024 sc1\n"
    "s_mov_b64 exec, 1\n"
    "buffer_atomic_add v12, off, s[44:47], s75 offset:0\n"
    "s_mov_b64 exec, -1\n"
    "s_add_u32 m0, s52, 0x1e000\n"
    "s_mul_i32 s55, s58, 0x38000\n"
    "s_add_u32 s55, s55, 0x800\n"
    "buffer_load_dwordx4 v6, s[32:35], s55 offen lds\n"
    "s_add_u32 m0, s52, 0x20000\n"
    "s_mul_i32 s54, s59, 0x38000\n"
    "s_add_u32 s54, s54, 0x800\n"
    "buffer_load_dwordx4 v6, s[28:31], s54 offen lds\n"
    "s_add_u32 m0, s52, 0x22000\n"
    "s_mul_i32 s55, s59, 0x38000\n"
    "s_add_u32 s55, s55, 0x800\n"
    "buffer_load_dwordx4 v6, s[32:35], s55 offen lds\n"
    "s_cbranch_vccnz .Lfz_RET_bY22\n"
    "s_barrier\n"
    ".Lfz_RET_bY22:\n"
    "v_mfma_f32_16x16x32_bf16 v[96:99], v[16:19], v[192:195], v[96:99]\n"
    "ds_read_b64_tr_b16 v[28:29], v3 offset:10240\n"
    "v_mfma_f32_16x16x32_bf16 v[100:103], v[16:19], v[196:199], v[100:103]\n"
    "ds_read_b64_tr_b16 v[30:31], v3 offset:10496\n"
    "v_mfma_f32_16x16x32_bf16 v[108:111], v[20:23], v[196:199], v[108:111]\n"
    "ds_read_b64_tr_b16 v[16:17], v3 offset:4096\n"
    "v_mfma_f32_16x16x32_bf16 v[104:107], v[20:23], v[192:195], v[104:107]\n"
    "ds_read_b64_tr_b16 v[18:19], v3 offset:4352\n"
    "v_mfma_f32_16x16x32_bf16 v[96:99], v[24:27], v[200:203], v[96:99]\n"
    "ds_read_b64_tr_b16 v[20:21], v3 offset:12288\n"
    "v_mfma_f32_16x16x32_bf16 v[100:103], v[24:27], v[204:207], v[100:103]\n"
    "ds_read_b64_tr_b16 v[22:23], v3 offset:12544\n"
    "s_waitcnt lgkmcnt(4)\n"
    "v_mfma_f32_16x16x32_bf16 v[108:111], v[28:31], v[204:207], v[108:111]\n"
    "ds_read_b64_tr_b16 v[24:25], v3 offset:6144\n"
    "v_mfma_f32_16x16x32_bf16 v[104:107], v[28:31], v[200:203], v[104:107]\n"
    "ds_read_b64_tr_b16 v[26:27], v3 offset:6400\n"
    "s_waitcnt lgkmcnt(4)\n"
    "v_mfma_f32_16x16x32_bf16 v[96:99], v[16:19], v[208:211], v[96:99]\n"
    "ds_read_b64_tr_b16 v[28:29], v3 offset:14336\n"
    "v_mfma_f32_16x16x32_bf16 v[100:103], v[16:19], v[212:215], v[100:103]\n"
    "ds_read_b64_tr_b16 v[30:31], v3 offset:14592\n"
    "s_waitcnt lgkmcnt(4)\n"
    "v_mfma_f32_16x16x32_bf16 v[108:111], v[20:23], v[212:215], v[108:111]\n"
    "ds_read_b64_tr_b16 v[16:17], v3 offset:16384\n"
    "v_mfma_f32_16x16x32_bf16 v[104:107], v[20:23], v[208:211], v[104:107]\n"
    "ds_read_b64_tr_b16 v[18:19], v3 offset:16640\n"
    "s_waitcnt lgkmcnt(4)\n"
    "v_mfma_f32_16x16x32_bf16 v[96:99], v[24:27], v[216:219], v[96:99]\n"
    "ds_read_b64_tr_b16 v[20:21], v3 offset:24576\n"
    "v_mfma_f32_16x16x32_bf16 v[100:103], v[24:27], v[220:223], v[100:103]\n"
    "ds_read_b64_tr_b16 v[22:23], v3 offset:24832\n"
    "s_waitcnt lgkmcnt(4)\n"
    "v_mfma_f32_16x16x32_bf16 v[108:111], v[28:31], v[220:223], v[108:111]\n"
    "ds_read_b64_tr_b16 v[24:25], v3 offset:18432\n"
    "v_mfma_f32_16x16x32_bf16 v[104:107], v[28:31], v[216:219], v[104:107]\n"
    "ds_read_b64_tr_b16 v[26:27], v3 offset:18688\n"
    "s_waitcnt vmcnt(10)\n"
    "s_cbranch_vccz .Lfz_RET_bY23\n"
    "s_barrier\n"
    ".Lfz_RET_bY23:\n"
    "s_add_u32 m0, s52, 0x24000\n"
    "s_mul_i32 s54, s60, 0x38000\n"
    "s_add_u32 s54, s54, 0x800\n"
    "buffer_load_dwordx4 v6, s[28:31], s54 offen lds\n"
    "s_add_u32 m0, s52, 0x26000\n"
    "s_mul_i32 s55, s60, 0x38000\n"
    "s_add_u32 s55, s55, 0x800\n"
    "buffer_load_dwordx4 v6, s[32:35], s55 offen lds\n"
    "s_add_u32 m0, s52, 0x10000\n"
    "s_mul_i32 s54, s61, 0x38000\n"
    "s_add_u32 s54, s54, 0x800\n"
    "buffer_load_dwordx4 v6, s[28:31], s54 offen lds\n"
    "s_add_u32 m0, s52, 0x12000\n"
    "s_mul_i32 s55, s61, 0x38000\n"
    "s_add_u32 s55, s55, 0x800\n"
    "buffer_load_dwordx4 v6, s[32:35], s55 offen lds\n"
    "s_cbranch_vccnz .Lfz_RET_bY24\n"
    "s_barrier\n"
    ".Lfz_RET_bY24:\n"
    "s_waitcnt lgkmcnt(4)\n"
    "v_mfma_f32_16x16x32_bf16 v[96:99], v[16:19], v[224:227], v[96:99]\n"
    "ds_read_b64_tr_b16 v[28:29], v3 offset:26624\n"
    "v_mfma_f32_16x16x32_bf16 v[100:103], v[16:19], v[228:231], v[100:103]\n"
    "ds_read_b64_tr_b16 v[30:31], v3 offset:26880\n"
    "s_waitcnt lgkmcnt(4)\n"
    "v_mfma_f32_16x16x32_bf16 v[108:111], v[20:23], v[228:231], v[108:111]\n"
    "ds_read_b64_tr_b16 v[16:17], v3 offset:20480\n"
    "v_mfma_f32_16x16x32_bf16 v[104:107], v[20:23], v[224:227], v[104:107]\n"
    "ds_read_b64_tr_b16 v[18:19], v3 offset:20736\n"
    "s_waitcnt lgkmcnt(4)\n"
    "v_mfma_f32_16x16x32_bf16 v[96:99], v[24:27], v[232:235], v[96:99]\n"
    "ds_read_b64_tr_b16 v[20:21], v3 offset:28672\n"
    "v_mfma_f32_16x16x32_bf16 v[100:103], v[24:27], v[236:239], v[100:103]\n"
    "ds_read_b64_tr_b16 v[22:23], v3 offset:28928\n"
    "s_waitcnt lgkmcnt(4)\n"
    "v_mfma_f32_16x16x32_bf16 v[108:111], v[28:31], v[236:239], v[108:111]\n"
    "ds_read_b64_tr_b16 v[24:25], v3 offset:22528\n"
    "v_mfma_f32_16x16x32_bf16 v[104:107], v[28:31], v[232:235], v[104:107]\n"
    "ds_read_b64_tr_b16 v[26:27], v3 offset:22784\n"
    "s_waitcnt lgkmcnt(4)\n"
    "v_mfma_f32_16x16x32_bf16 v[96:99], v[16:19], v[240:243], v[96:99]\n"
    "ds_read_b64_tr_b16 v[28:29], v3 offset:30720\n"
    "v_mfma_f32_16x16x32_bf16 v[100:103], v[16:19], v[244:247], v[100:103]\n"
    "ds_read_b64_tr_b16 v[30:31], v3 offset:30976\n"
    "s_waitcnt lgkmcnt(4)\n"
    "v_mfma_f32_16x16x32_bf16 v[108:111], v[20:23], v[244:247], v[108:111]\n"
    "v_add_u32 v1, 0xffff8000, v1\n"
    "ds_read_b64_tr_b16 v[16:17], v1 offset:0\n"
    "v_mfma_f32_16x16x32_bf16 v[104:107], v[20:23], v[240:243], v[104:107]\n"
    "ds_read_b64_tr_b16 v[18:19], v1 offset:512\n"
    "s_waitcnt lgkmcnt(4)\n"
    "v_mfma_f32_16x16x32_bf16 v[96:99], v[24:27], v[248:251], v[96:99]\n"
    "v_add_u32 v2, 0xffff8000, v2\n"
    "ds_read_b64_tr_b16 v[20:21], v2 offset:0\n"
    "v_mfma_f32_16x16x32_bf16 v[100:103], v[24:27], v[252:255], v[100:103]\n"
    "ds_read_b64_tr_b16 v[22:23], v2 offset:512\n"
    "s_waitcnt lgkmcnt(4)\n"
    "v_mfma_f32_16x16x32_bf16 v[108:111], v[28:31], v[252:255], v[108:111]\n"
    "ds_read_b64_tr_b16 v[24:25], v1 offset:8192\n"
    "v_mfma_f32_16x16x32_bf16 v[104:107], v[28:31], v[248:251], v[104:107]\n"
    "ds_read_b64_tr_b16 v[26:27], v1 offset:8704\n"
    "s_waitcnt vmcnt(12)\n"
    "s_cbranch_vccz .Lfz_RET_bY25\n"
    "s_barrier\n"
    ".Lfz_RET_bY25:\n"
    "s_add_u32 m0, s52, 0x14000\n"
    "s_mul_i32 s54, s62, 0x38000\n"
    "s_add_u32 s54, s54, 0x800\n"
    "buffer_load_dwordx4 v6, s[28:31], s54 offen lds\n"
    "v_mov_b32 v40, 0xbdd2d3e8\n"
    "v_mov_b32 v42, 0xc0135761\n"
    "v_lshlrev_b32 v41, 3, v0\n"
    "v_sub_u32 v41, v10, v41\n"
    "v_mul_f32 v32, v96, v96\n"
    "v_mul_f32 v33, v97, v97\n"
    "v_mul_f32 v34, v98, v98\n"
    "v_mul_f32 v35, v99, v99\n"
    "v_fmaak_f32 v32, v40, v32, 0xc0135761\n"
    "s_add_u32 m0, s52, 0x16000\n"
    "s_mul_i32 s55, s62, 0x38000\n"
    "s_add_u32 s55, s55, 0x800\n"
    "buffer_load_dwordx4 v6, s[32:35], s55 offen lds\n"
    "v_fmaak_f32 v33, v40, v33, 0xc0135761\n"
    "v_fmaak_f32 v34, v40, v34, 0xc0135761\n"
    "v_fmaak_f32 v35, v40, v35, 0xc0135761\n"
    "v_mul_f32 v32, v32, v96\n"
    "v_mul_f32 v33, v33, v97\n"
    "v_mul_f32 v34, v34, v98\n"
    "v_mul_f32 v35, v35, v99\n"
    "v_exp_f32 v32, v32\n"
    "s_add_u32 m0, s52, 0x18000\n"
    "s_mul_i32 s54, s63, 0x38000\n"
    "s_add_u32 s54, s54, 0x800\n"
    "buffer_load_dwordx4 v6, s[28:31], s54 offen lds\n"
    "v_mul_f32 v104, v104, v96\n"
    "v_exp_f32 v33, v33\n"
    "v_mul_f32 v105, v105, v97\n"
    "v_exp_f32 v34, v34\n"
    "v_mul_f32 v106, v106, v98\n"
    "v_exp_f32 v35, v35\n"
    "s_add_u32 m0, s52, 0x1a000\n"
    "s_mul_i32 s55, s63, 0x38000\n"
    "s_add_u32 s55, s55, 0x800\n"
    "buffer_load_dwordx4 v6, s[32:35], s55 offen lds\n"
    "v_mul_f32 v107, v107, v99\n"
    "v_add_f32 v32, 1.0, v32\n"
    "v_add_f32 v33, 1.0, v33\n"
    "v_add_f32 v34, 1.0, v34\n"
    "v_add_f32 v35, 1.0, v35\n"
    "v_rcp_f32 v32, v32\n"
    "v_rcp_f32 v33, v33\n"
    "s_cbranch_vccnz .Lfz_RET_bY26\n"
    "s_barrier\n"
    ".Lfz_RET_bY26:\n"
    "s_waitcnt lgkmcnt(4)\n"
    "v_mfma_f32_16x16x32_bf16 v[112:115], v[16:19], v[128:131], 0\n"
    "ds_read_b64_tr_b16 v[28:29], v2 offset:8192\n"
    "v_mfma_f32_16x16x32_bf16 v[116:119], v[16:19], v[132:135], 0\n"
    "ds_read_b64_tr_b16 v[30:31], v2 offset:8704\n"
    "s_waitcnt lgkmcnt(4)\n"
    "v_mfma_f32_16x16x32_bf16 v[124:127], v[20:23], v[132:135], 0\n"
    "ds_read_b64_tr_b16 v[16:17], v1 offset:4096\n"
    "v_mfma_f32_16x16x32_bf16 v[120:123], v[20:23], v[128:131], 0\n"
    "ds_read_b64_tr_b16 v[18:19], v1 offset:4608\n"
    "s_waitcnt lgkmcnt(4)\n"
    "v_mfma_f32_16x16x32_bf16 v[64:67], v[24:27], v[128:131], 0\n"
    "ds_read_b64_tr_b16 v[20:21], v2 offset:4096\n"
    "v_mfma_f32_16x16x32_bf16 v[68:71], v[24:27], v[132:135], 0\n"
    "ds_read_b64_tr_b16 v[22:23], v2 offset:4608\n"
    "s_waitcnt lgkmcnt(4)\n"
    "v_mfma_f32_16x16x32_bf16 v[76:79], v[28:31], v[132:135], 0\n"
    "ds_read_b64_tr_b16 v[24:25], v1 offset:12288\n"
    "v_mfma_f32_16x16x32_bf16 v[72:75], v[28:31], v[128:131], 0\n"
    "ds_read_b64_tr_b16 v[26:27], v1 offset:12800\n"
    "s_waitcnt lgkmcnt(4)\n"
    "v_mfma_f32_16x16x32_bf16 v[112:115], v[16:19], v[136:139], v[112:115]\n"
    "ds_read_b64_tr_b16 v[28:29], v2 offset:12288\n"
    "v_mfma_f32_16x16x32_bf16 v[116:119], v[16:19], v[140:143], v[116:119]\n"
    "ds_read_b64_tr_b16 v[30:31], v2 offset:12800\n"
    "s_waitcnt lgkmcnt(4)\n"
    "v_mfma_f32_16x16x32_bf16 v[124:127], v[20:23], v[140:143], v[124:127]\n"
    "ds_read_b64_tr_b16 v[16:17], v1 offset:16384\n"
    "v_mfma_f32_16x16x32_bf16 v[120:123], v[20:23], v[136:139], v[120:123]\n"
    "ds_read_b64_tr_b16 v[18:19], v1 offset:16896\n"
    "s_waitcnt lgkmcnt(4)\n"
    "v_mfma_f32_16x16x32_bf16 v[64:67], v[24:27], v[136:139], v[64:67]\n"
    "ds_read_b64_tr_b16 v[20:21], v2 offset:16384\n"
    "v_mfma_f32_16x16x32_bf16 v[68:71], v[24:27], v[140:143], v[68:71]\n"
    "ds_read_b64_tr_b16 v[22:23], v2 offset:16896\n"
    "s_waitcnt lgkmcnt(4)\n"
    "v_mfma_f32_16x16x32_bf16 v[76:79], v[28:31], v[140:143], v[76:79]\n"
    "ds_read_b64_tr_b16 v[24:25], v1 offset:24576\n"
    "v_mfma_f32_16x16x32_bf16 v[72:75], v[28:31], v[136:139], v[72:75]\n"
    "ds_read_b64_tr_b16 v[26:27], v1 offset:25088\n"
    "s_waitcnt vmcnt(10)\n"
    "s_cbranch_vccz .Lfz_RET_bY27\n"
    "s_barrier\n"
    ".Lfz_RET_bY27:\n"
    "s_add_u32 m0, s52, 0x0\n"
    "s_mov_b32 s54, 0x0\n"
    "buffer_load_dwordx4 v9, s[36:39], s54 offen lds\n"
    "v_rcp_f32 v34, v34\n"
    "v_rcp_f32 v35, v35\n"
    "v_mul_f32 v32, v32, v104\n"
    "v_mul_f32 v33, v33, v105\n"
    "v_mul_f32 v34, v34, v106\n"
    "v_mul_f32 v35, v35, v107\n"
    "v_cvt_pk_bf16_f32 v36, v32, v33\n"
    "v_cvt_pk_bf16_f32 v37, v34, v35\n"
    "s_add_u32 m0, s52, 0x2000\n"
    "s_mov_b32 s55, 0x10000\n"
    "buffer_load_dwordx4 v9, s[36:39], s55 offen lds\n"
    "buffer_store_dwordx2 v[36:37], v41, s[44:47], s68 offen offset:0 sc1\n"
    "v_mul_f32 v32, v100, v100\n"
    "v_mul_f32 v33, v101, v101\n"
    "v_mul_f32 v34, v102, v102\n"
    "v_mul_f32 v35, v103, v103\n"
    "v_fmaak_f32 v32, v40, v32, 0xc0135761\n"
    "v_fmaak_f32 v33, v40, v33, 0xc0135761\n"
    "v_fmaak_f32 v34, v40, v34, 0xc0135761\n"
    "v_fmaak_f32 v35, v40, v35, 0xc0135761\n"
    "s_add_u32 m0, s52, 0x4000\n"
    "s_mov_b32 s54, 0x20000\n"
    "buffer_load_dwordx4 v9, s[36:39], s54 offen lds\n"
    "v_mul_f32 v32, v32, v100\n"
    "v_mul_f32 v33, v33, v101\n"
    "v_mul_f32 v34, v34, v102\n"
    "v_mul_f32 v35, v35, v103\n"
    "v_exp_f32 v32, v32\n"
    "v_mul_f32 v108, v108, v100\n"
    "v_exp_f32 v33, v33\n"
    "s_add_u32 m0, s52, 0x6000\n"
    "s_mov_b32 s55, 0x30000\n"
    "buffer_load_dwordx4 v9, s[36:39], s55 offen lds\n"
    "v_mul_f32 v109, v109, v101\n"
    "v_exp_f32 v34, v34\n"
    "v_mul_f32 v110, v110, v102\n"
    "v_exp_f32 v35, v35\n"
    "v_mul_f32 v111, v111, v103\n"
    "v_add_f32 v32, 1.0, v32\n"
    "v_add_f32 v33, 1.0, v33\n"
    "s_cbranch_vccnz .Lfz_RET_bY28\n"
    "s_barrier\n"
    ".Lfz_RET_bY28:\n"
    "s_waitcnt lgkmcnt(4)\n"
    "v_mfma_f32_16x16x32_bf16 v[112:115], v[16:19], v[144:147], v[112:115]\n"
    "ds_read_b64_tr_b16 v[28:29], v2 offset:24576\n"
    "v_mfma_f32_16x16x32_bf16 v[116:119], v[16:19], v[148:151], v[116:119]\n"
    "ds_read_b64_tr_b16 v[30:31], v2 offset:25088\n"
    "s_waitcnt lgkmcnt(4)\n"
    "v_mfma_f32_16x16x32_bf16 v[124:127], v[20:23], v[148:151], v[124:127]\n"
    "ds_read_b64_tr_b16 v[16:17], v1 offset:20480\n"
    "v_mfma_f32_16x16x32_bf16 v[120:123], v[20:23], v[144:147], v[120:123]\n"
    "ds_read_b64_tr_b16 v[18:19], v1 offset:20992\n"
    "s_waitcnt lgkmcnt(4)\n"
    "v_mfma_f32_16x16x32_bf16 v[64:67], v[24:27], v[144:147], v[64:67]\n"
    "ds_read_b64_tr_b16 v[20:21], v2 offset:20480\n"
    "v_mfma_f32_16x16x32_bf16 v[68:71], v[24:27], v[148:151], v[68:71]\n"
    "ds_read_b64_tr_b16 v[22:23], v2 offset:20992\n"
    "s_waitcnt lgkmcnt(4)\n"
    "v_mfma_f32_16x16x32_bf16 v[76:79], v[28:31], v[148:151], v[76:79]\n"
    "ds_read_b64_tr_b16 v[24:25], v1 offset:28672\n"
    "v_mfma_f32_16x16x32_bf16 v[72:75], v[28:31], v[144:147], v[72:75]\n"
    "ds_read_b64_tr_b16 v[26:27], v1 offset:29184\n"
    "s_waitcnt lgkmcnt(4)\n"
    "v_mfma_f32_16x16x32_bf16 v[112:115], v[16:19], v[152:155], v[112:115]\n"
    "ds_read_b64_tr_b16 v[28:29], v2 offset:28672\n"
    "v_mfma_f32_16x16x32_bf16 v[116:119], v[16:19], v[156:159], v[116:119]\n"
    "ds_read_b64_tr_b16 v[30:31], v2 offset:29184\n"
    "s_waitcnt lgkmcnt(4)\n"
    "v_mfma_f32_16x16x32_bf16 v[124:127], v[20:23], v[156:159], v[124:127]\n"
    "v_add_u32 v1, 0x14000, v1\n"
    "ds_read_b64_tr_b16 v[16:17], v1 offset:0\n"
    "v_mfma_f32_16x16x32_bf16 v[120:123], v[20:23], v[152:155], v[120:123]\n"
    "ds_read_b64_tr_b16 v[18:19], v1 offset:512\n"
    "s_waitcnt lgkmcnt(4)\n"
    "v_mfma_f32_16x16x32_bf16 v[64:67], v[24:27], v[152:155], v[64:67]\n"
    "v_add_u32 v2, 0x14000, v2\n"
    "ds_read_b64_tr_b16 v[20:21], v2 offset:0\n"
    "v_mfma_f32_16x16x32_bf16 v[68:71], v[24:27], v[156:159], v[68:71]\n"
    "ds_read_b64_tr_b16 v[22:23], v2 offset:512\n"
    "s_waitcnt lgkmcnt(4)\n"
    "v_mfma_f32_16x16x32_bf16 v[76:79], v[28:31], v[156:159], v[76:79]\n"
    "ds_read_b64_tr_b16 v[24:25], v1 offset:8192\n"
    "v_mfma_f32_16x16x32_bf16 v[72:75], v[28:31], v[152:155], v[72:75]\n"
    "ds_read_b64_tr_b16 v[26:27], v1 offset:8704\n"
    "s_waitcnt vmcnt(13)\n"
    "s_cbranch_vccz .Lfz_RET_bY29\n"
    "s_barrier\n"
    ".Lfz_RET_bY29:\n"
    "s_add_u32 m0, s52, 0x8000\n"
    "s_mov_b32 s54, 0x40000\n"
    "buffer_load_dwordx4 v9, s[36:39], s54 offen lds\n"
    "v_add_f32 v34, 1.0, v34\n"
    "v_add_f32 v35, 1.0, v35\n"
    "v_rcp_f32 v32, v32\n"
    "v_rcp_f32 v33, v33\n"
    "v_rcp_f32 v34, v34\n"
    "v_rcp_f32 v35, v35\n"
    "s_add_u32 m0, s52, 0xa000\n"
    "s_mov_b32 s55, 0x50000\n"
    "buffer_load_dwordx4 v9, s[36:39], s55 offen lds\n"
    "v_mul_f32 v32, v32, v108\n"
    "v_mul_f32 v33, v33, v109\n"
    "v_mul_f32 v34, v34, v110\n"
    "v_mul_f32 v35, v35, v111\n"
    "v_cvt_pk_bf16_f32 v38, v32, v33\n"
    "v_cvt_pk_bf16_f32 v39, v34, v35\n"
    "buffer_store_dwordx2 v[38:39], v41, s[44:47], s68 offen offset:1024 sc1\n"
    "s_cbranch_vccnz .Lfz_RET_bY30\n"
    "s_barrier\n"
    ".Lfz_RET_bY30:\n"
    "s_waitcnt lgkmcnt(4)\n"
    "v_mfma_f32_16x16x32_bf16 v[112:115], v[16:19], v[160:163], v[112:115]\n"
    "ds_read_b64_tr_b16 v[28:29], v2 offset:8192\n"
    "v_mfma_f32_16x16x32_bf16 v[116:119], v[16:19], v[164:167], v[116:119]\n"
    "ds_read_b64_tr_b16 v[30:31], v2 offset:8704\n"
    "s_waitcnt lgkmcnt(4)\n"
    "v_mfma_f32_16x16x32_bf16 v[124:127], v[20:23], v[164:167], v[124:127]\n"
    "ds_read_b64_tr_b16 v[16:17], v1 offset:4096\n"
    "v_mfma_f32_16x16x32_bf16 v[120:123], v[20:23], v[160:163], v[120:123]\n"
    "ds_read_b64_tr_b16 v[18:19], v1 offset:4608\n"
    "s_waitcnt lgkmcnt(4)\n"
    "v_mfma_f32_16x16x32_bf16 v[64:67], v[24:27], v[160:163], v[64:67]\n"
    "ds_read_b64_tr_b16 v[20:21], v2 offset:4096\n"
    "v_mfma_f32_16x16x32_bf16 v[68:71], v[24:27], v[164:167], v[68:71]\n"
    "ds_read_b64_tr_b16 v[22:23], v2 offset:4608\n"
    "s_waitcnt lgkmcnt(4)\n"
    "v_mfma_f32_16x16x32_bf16 v[76:79], v[28:31], v[164:167], v[76:79]\n"
    "ds_read_b64_tr_b16 v[24:25], v1 offset:12288\n"
    "v_mfma_f32_16x16x32_bf16 v[72:75], v[28:31], v[160:163], v[72:75]\n"
    "ds_read_b64_tr_b16 v[26:27], v1 offset:12800\n"
    "s_waitcnt lgkmcnt(4)\n"
    "v_mfma_f32_16x16x32_bf16 v[112:115], v[16:19], v[168:171], v[112:115]\n"
    "ds_read_b64_tr_b16 v[28:29], v2 offset:12288\n"
    "v_mfma_f32_16x16x32_bf16 v[116:119], v[16:19], v[172:175], v[116:119]\n"
    "ds_read_b64_tr_b16 v[30:31], v2 offset:12800\n"
    "s_waitcnt lgkmcnt(4)\n"
    "v_mfma_f32_16x16x32_bf16 v[124:127], v[20:23], v[172:175], v[124:127]\n"
    "ds_read_b64_tr_b16 v[16:17], v1 offset:16384\n"
    "v_mfma_f32_16x16x32_bf16 v[120:123], v[20:23], v[168:171], v[120:123]\n"
    "ds_read_b64_tr_b16 v[18:19], v1 offset:16896\n"
    "s_waitcnt lgkmcnt(4)\n"
    "v_mfma_f32_16x16x32_bf16 v[64:67], v[24:27], v[168:171], v[64:67]\n"
    "ds_read_b64_tr_b16 v[20:21], v2 offset:16384\n"
    "v_mfma_f32_16x16x32_bf16 v[68:71], v[24:27], v[172:175], v[68:71]\n"
    "ds_read_b64_tr_b16 v[22:23], v2 offset:16896\n"
    "s_waitcnt lgkmcnt(4)\n"
    "v_mfma_f32_16x16x32_bf16 v[76:79], v[28:31], v[172:175], v[76:79]\n"
    "ds_read_b64_tr_b16 v[24:25], v1 offset:24576\n"
    "v_mfma_f32_16x16x32_bf16 v[72:75], v[28:31], v[168:171], v[72:75]\n"
    "ds_read_b64_tr_b16 v[26:27], v1 offset:25088\n"
    "s_waitcnt vmcnt(14)\n"
    "s_cbranch_vccz .Lfz_RET_bY31\n"
    "s_barrier\n"
    ".Lfz_RET_bY31:\n"
    "s_add_u32 m0, s52, 0xc000\n"
    "s_mov_b32 s54, 0x60000\n"
    "buffer_load_dwordx4 v9, s[36:39], s54 offen lds\n"
    "s_waitcnt vmcnt(1)\n"
    "s_mov_b64 exec, 1\n"
    "buffer_atomic_add v12, off, s[44:47], s65 offset:4\n"
    "s_mov_b64 exec, -1\n"
    "v_mov_b32 v14, 0\n"
    "s_mov_b64 exec, 1\n"
    "buffer_atomic_or v14, off, s[44:47], s6 offset:0 sc0\n"
    "s_mov_b64 exec, -1\n"
    "s_add_u32 m0, s52, 0xe000\n"
    "s_mov_b32 s55, 0x70000\n"
    "buffer_load_dwordx4 v9, s[36:39], s55 offen lds\n"
    "s_cbranch_vccnz .Lfz_RET_bY32\n"
    "s_barrier\n"
    ".Lfz_RET_bY32:\n"
    "s_waitcnt lgkmcnt(4)\n"
    "v_mfma_f32_16x16x32_bf16 v[112:115], v[16:19], v[176:179], v[112:115]\n"
    "ds_read_b64_tr_b16 v[28:29], v2 offset:24576\n"
    "v_mfma_f32_16x16x32_bf16 v[116:119], v[16:19], v[180:183], v[116:119]\n"
    "ds_read_b64_tr_b16 v[30:31], v2 offset:25088\n"
    "s_waitcnt lgkmcnt(4)\n"
    "v_mfma_f32_16x16x32_bf16 v[124:127], v[20:23], v[180:183], v[124:127]\n"
    "ds_read_b64_tr_b16 v[16:17], v1 offset:20480\n"
    "v_mfma_f32_16x16x32_bf16 v[120:123], v[20:23], v[176:179], v[120:123]\n"
    "ds_read_b64_tr_b16 v[18:19], v1 offset:20992\n"
    "s_waitcnt lgkmcnt(4)\n"
    "v_mfma_f32_16x16x32_bf16 v[64:67], v[24:27], v[176:179], v[64:67]\n"
    "ds_read_b64_tr_b16 v[20:21], v2 offset:20480\n"
    "v_mfma_f32_16x16x32_bf16 v[68:71], v[24:27], v[180:183], v[68:71]\n"
    "ds_read_b64_tr_b16 v[22:23], v2 offset:20992\n"
    "s_waitcnt lgkmcnt(4)\n"
    "v_mfma_f32_16x16x32_bf16 v[76:79], v[28:31], v[180:183], v[76:79]\n"
    "ds_read_b64_tr_b16 v[24:25], v1 offset:28672\n"
    "v_mfma_f32_16x16x32_bf16 v[72:75], v[28:31], v[176:179], v[72:75]\n"
    "ds_read_b64_tr_b16 v[26:27], v1 offset:29184\n"
    "s_waitcnt lgkmcnt(4)\n"
    "v_mfma_f32_16x16x32_bf16 v[112:115], v[16:19], v[184:187], v[112:115]\n"
    "ds_read_b64_tr_b16 v[28:29], v2 offset:28672\n"
    "v_mfma_f32_16x16x32_bf16 v[116:119], v[16:19], v[188:191], v[116:119]\n"
    "ds_read_b64_tr_b16 v[30:31], v2 offset:29184\n"
    "s_waitcnt lgkmcnt(4)\n"
    "v_mfma_f32_16x16x32_bf16 v[124:127], v[20:23], v[188:191], v[124:127]\n"
    "ds_read_b64_tr_b16 v[16:17], v1 offset:32768\n"
    "v_mfma_f32_16x16x32_bf16 v[120:123], v[20:23], v[184:187], v[120:123]\n"
    "ds_read_b64_tr_b16 v[18:19], v1 offset:33280\n"
    "s_waitcnt lgkmcnt(4)\n"
    "v_mfma_f32_16x16x32_bf16 v[64:67], v[24:27], v[184:187], v[64:67]\n"
    "ds_read_b64_tr_b16 v[20:21], v2 offset:32768\n"
    "v_mfma_f32_16x16x32_bf16 v[68:71], v[24:27], v[188:191], v[68:71]\n"
    "ds_read_b64_tr_b16 v[22:23], v2 offset:33280\n"
    "s_waitcnt lgkmcnt(4)\n"
    "v_mfma_f32_16x16x32_bf16 v[76:79], v[28:31], v[188:191], v[76:79]\n"
    "ds_read_b64_tr_b16 v[24:25], v1 offset:40960\n"
    "v_mfma_f32_16x16x32_bf16 v[72:75], v[28:31], v[184:187], v[72:75]\n"
    "ds_read_b64_tr_b16 v[26:27], v1 offset:41472\n"
    "s_cbranch_vccz .Lfz_RET_bY33\n"
    "s_barrier\n"
    ".Lfz_RET_bY33:\n"
    "s_add_u32 m0, s52, 0x1c000\n"
    "s_mov_b32 s54, 0x180000\n"
    "buffer_load_dwordx4 v9, s[36:39], s54 offen lds\n"
    "s_waitcnt vmcnt(2)\n"
    "v_readfirstlane_b32 s90, v14\n"
    "s_sub_u32 s90, s90, s53\n"
    "s_cmp_eq_u32 s90, 4\n"
    "s_cbranch_scc0 .Lfz_SLOW_Y_1\n"
    ".Lfz_RET_Y_1:\n"
    "s_add_u32 m0, s52, 0x1e000\n"
    "s_mov_b32 s55, 0x190000\n"
    "buffer_load_dwordx4 v9, s[36:39], s55 offen lds\n"
    "s_mov_b32 s88, 0x0\n"
    "buffer_load_dwordx4 v[128:131], v11, s[44:47], s88 offen offset:0 sc1\n"
    "s_mov_b32 s88, 0x0\n"
    "buffer_load_dwordx4 v[132:135], v11, s[44:47], s88 offen offset:1024 sc1\n"
    "s_cbranch_vccnz .Lfz_RET_bY34\n"
    "s_barrier\n"
    ".Lfz_RET_bY34:\n"
    "s_waitcnt lgkmcnt(4)\n"
    "v_mfma_f32_16x16x32_bf16 v[112:115], v[16:19], v[192:195], v[112:115]\n"
    "ds_read_b64_tr_b16 v[28:29], v2 offset:40960\n"
    "v_mfma_f32_16x16x32_bf16 v[116:119], v[16:19], v[196:199], v[116:119]\n"
    "ds_read_b64_tr_b16 v[30:31], v2 offset:41472\n"
    "s_waitcnt lgkmcnt(4)\n"
    "v_mfma_f32_16x16x32_bf16 v[124:127], v[20:23], v[196:199], v[124:127]\n"
    "ds_read_b64_tr_b16 v[16:17], v1 offset:36864\n"
    "v_mfma_f32_16x16x32_bf16 v[120:123], v[20:23], v[192:195], v[120:123]\n"
    "ds_read_b64_tr_b16 v[18:19], v1 offset:37376\n"
    "s_waitcnt lgkmcnt(4)\n"
    "v_mfma_f32_16x16x32_bf16 v[64:67], v[24:27], v[192:195], v[64:67]\n"
    "ds_read_b64_tr_b16 v[20:21], v2 offset:36864\n"
    "v_mfma_f32_16x16x32_bf16 v[68:71], v[24:27], v[196:199], v[68:71]\n"
    "ds_read_b64_tr_b16 v[22:23], v2 offset:37376\n"
    "s_waitcnt lgkmcnt(4)\n"
    "v_mfma_f32_16x16x32_bf16 v[76:79], v[28:31], v[196:199], v[76:79]\n"
    "ds_read_b64_tr_b16 v[24:25], v1 offset:45056\n"
    "v_mfma_f32_16x16x32_bf16 v[72:75], v[28:31], v[192:195], v[72:75]\n"
    "ds_read_b64_tr_b16 v[26:27], v1 offset:45568\n"
    "s_waitcnt lgkmcnt(4)\n"
    "v_mfma_f32_16x16x32_bf16 v[112:115], v[16:19], v[200:203], v[112:115]\n"
    "ds_read_b64_tr_b16 v[28:29], v2 offset:45056\n"
    "v_mfma_f32_16x16x32_bf16 v[116:119], v[16:19], v[204:207], v[116:119]\n"
    "ds_read_b64_tr_b16 v[30:31], v2 offset:45568\n"
    "s_waitcnt lgkmcnt(4)\n"
    "v_mfma_f32_16x16x32_bf16 v[124:127], v[20:23], v[204:207], v[124:127]\n"
    "v_add_u32 v1, 0xffff4000, v1\n"
    "ds_read_b64_tr_b16 v[16:17], v1 offset:0\n"
    "v_mfma_f32_16x16x32_bf16 v[120:123], v[20:23], v[200:203], v[120:123]\n"
    "ds_read_b64_tr_b16 v[18:19], v1 offset:512\n"
    "s_waitcnt lgkmcnt(4)\n"
    "v_mfma_f32_16x16x32_bf16 v[64:67], v[24:27], v[200:203], v[64:67]\n"
    "v_add_u32 v2, 0xffff4000, v2\n"
    "ds_read_b64_tr_b16 v[20:21], v2 offset:0\n"
    "v_mfma_f32_16x16x32_bf16 v[68:71], v[24:27], v[204:207], v[68:71]\n"
    "ds_read_b64_tr_b16 v[22:23], v2 offset:512\n"
    "s_waitcnt lgkmcnt(4)\n"
    "v_mfma_f32_16x16x32_bf16 v[76:79], v[28:31], v[204:207], v[76:79]\n"
    "ds_read_b64_tr_b16 v[24:25], v1 offset:8192\n"
    "v_mfma_f32_16x16x32_bf16 v[72:75], v[28:31], v[200:203], v[72:75]\n"
    "ds_read_b64_tr_b16 v[26:27], v1 offset:8704\n"
    "s_cbranch_vccz .Lfz_RET_bY35\n"
    "s_barrier\n"
    ".Lfz_RET_bY35:\n"
    "s_add_u32 m0, s52, 0x20000\n"
    "s_mov_b32 s54, 0x1a0000\n"
    "buffer_load_dwordx4 v9, s[36:39], s54 offen lds\n"
    "v_bfe_u32 v7, v0, 3, 2\n"
    "v_xor_b32 v7, s85, v7\n"
    "v_and_b32 v8, 3, v0\n"
    "v_lshlrev_b32 v8, 3, v8\n"
    "v_lshl_or_b32 v7, v7, 5, v8\n"
    "v_lshrrev_b32 v8, 2, v0\n"
    "v_lshl_or_b32 v7, v8, 7, v7\n"
    "s_lshl_b32 s87, s85, 12\n"
    "v_add_u32 v6, s87, v7\n"
    "v_xor_b32 v7, 32, v6\n"
    "v_xor_b32 v8, 64, v6\n"
    "v_xor_b32 v4, 0x60, v6\n"
    "s_add_u32 m0, s52, 0x22000\n"
    "s_mov_b32 s55, 0x1b0000\n"
    "buffer_load_dwordx4 v9, s[36:39], s55 offen lds\n"
    "s_mov_b32 s88, 0x2000\n"
    "buffer_load_dwordx4 v[136:139], v11, s[44:47], s88 offen offset:0 sc1\n"
    "s_mov_b32 s88, 0x2000\n"
    "buffer_load_dwordx4 v[140:143], v11, s[44:47], s88 offen offset:1024 sc1\n"
    "s_cbranch_vccnz .Lfz_RET_bY36\n"
    "s_barrier\n"
    ".Lfz_RET_bY36:\n"
    "s_waitcnt lgkmcnt(4)\n"
    "v_mfma_f32_16x16x32_bf16 v[112:115], v[16:19], v[208:211], v[112:115]\n"
    "ds_read_b64_tr_b16 v[28:29], v2 offset:8192\n"
    "v_mfma_f32_16x16x32_bf16 v[116:119], v[16:19], v[212:215], v[116:119]\n"
    "ds_read_b64_tr_b16 v[30:31], v2 offset:8704\n"
    "s_waitcnt lgkmcnt(4)\n"
    "v_mfma_f32_16x16x32_bf16 v[124:127], v[20:23], v[212:215], v[124:127]\n"
    "ds_read_b64_tr_b16 v[16:17], v1 offset:4096\n"
    "v_mfma_f32_16x16x32_bf16 v[120:123], v[20:23], v[208:211], v[120:123]\n"
    "ds_read_b64_tr_b16 v[18:19], v1 offset:4608\n"
    "s_waitcnt lgkmcnt(4)\n"
    "v_mfma_f32_16x16x32_bf16 v[64:67], v[24:27], v[208:211], v[64:67]\n"
    "ds_read_b64_tr_b16 v[20:21], v2 offset:4096\n"
    "v_mfma_f32_16x16x32_bf16 v[68:71], v[24:27], v[212:215], v[68:71]\n"
    "ds_read_b64_tr_b16 v[22:23], v2 offset:4608\n"
    "s_waitcnt lgkmcnt(4)\n"
    "v_mfma_f32_16x16x32_bf16 v[76:79], v[28:31], v[212:215], v[76:79]\n"
    "ds_read_b64_tr_b16 v[24:25], v1 offset:12288\n"
    "v_mfma_f32_16x16x32_bf16 v[72:75], v[28:31], v[208:211], v[72:75]\n"
    "ds_read_b64_tr_b16 v[26:27], v1 offset:12800\n"
    "s_waitcnt lgkmcnt(4)\n"
    "v_mfma_f32_16x16x32_bf16 v[112:115], v[16:19], v[216:219], v[112:115]\n"
    "ds_read_b64_tr_b16 v[28:29], v2 offset:12288\n"
    "v_mfma_f32_16x16x32_bf16 v[116:119], v[16:19], v[220:223], v[116:119]\n"
    "ds_read_b64_tr_b16 v[30:31], v2 offset:12800\n"
    "s_waitcnt lgkmcnt(4)\n"
    "v_mfma_f32_16x16x32_bf16 v[124:127], v[20:23], v[220:223], v[124:127]\n"
    "ds_read_b64_tr_b16 v[16:17], v1 offset:16384\n"
    "v_mfma_f32_16x16x32_bf16 v[120:123], v[20:23], v[216:219], v[120:123]\n"
    "ds_read_b64_tr_b16 v[18:19], v1 offset:16896\n"
    "s_waitcnt lgkmcnt(4)\n"
    "v_mfma_f32_16x16x32_bf16 v[64:67], v[24:27], v[216:219], v[64:67]\n"
    "ds_read_b64_tr_b16 v[20:21], v2 offset:16384\n"
    "v_mfma_f32_16x16x32_bf16 v[68:71], v[24:27], v[220:223], v[68:71]\n"
    "ds_read_b64_tr_b16 v[22:23], v2 offset:16896\n"
    "s_waitcnt lgkmcnt(4)\n"
    "v_mfma_f32_16x16x32_bf16 v[76:79], v[28:31], v[220:223], v[76:79]\n"
    "ds_read_b64_tr_b16 v[24:25], v1 offset:24576\n"
    "v_mfma_f32_16x16x32_bf16 v[72:75], v[28:31], v[216:219], v[72:75]\n"
    "ds_read_b64_tr_b16 v[26:27], v1 offset:25088\n"
    "s_cbranch_vccz .Lfz_RET_bY37\n"
    "s_barrier\n"
    ".Lfz_RET_bY37:\n"
    "s_add_u32 m0, s52, 0x24000\n"
    "s_mov_b32 s54, 0x80000\n"
    "buffer_load_dwordx4 v9, s[36:39], s54 offen lds\n"
    "s_add_u32 m0, s52, 0x26000\n"
    "s_mov_b32 s55, 0x90000\n"
    "buffer_load_dwordx4 v9, s[36:39], s55 offen lds\n"
    "s_mov_b32 s88, 0x4000\n"
    "buffer_load_dwordx4 v[144:147], v11, s[44:47], s88 offen offset:0 sc1\n"
    "s_mov_b32 s88, 0x4000\n"
    "buffer_load_dwordx4 v[148:151], v11, s[44:47], s88 offen offset:1024 sc1\n"
    "s_cbranch_vccnz .Lfz_RET_bY38\n"
    "s_barrier\n"
    ".Lfz_RET_bY38:\n"
    "s_waitcnt lgkmcnt(4)\n"
    "v_mfma_f32_16x16x32_bf16 v[112:115], v[16:19], v[224:227], v[112:115]\n"
    "ds_read_b64_tr_b16 v[28:29], v2 offset:24576\n"
    "v_mfma_f32_16x16x32_bf16 v[116:119], v[16:19], v[228:231], v[116:119]\n"
    "ds_read_b64_tr_b16 v[30:31], v2 offset:25088\n"
    "s_waitcnt lgkmcnt(4)\n"
    "v_mfma_f32_16x16x32_bf16 v[124:127], v[20:23], v[228:231], v[124:127]\n"
    "ds_read_b64_tr_b16 v[16:17], v1 offset:20480\n"
    "v_mfma_f32_16x16x32_bf16 v[120:123], v[20:23], v[224:227], v[120:123]\n"
    "ds_read_b64_tr_b16 v[18:19], v1 offset:20992\n"
    "s_waitcnt lgkmcnt(4)\n"
    "v_mfma_f32_16x16x32_bf16 v[64:67], v[24:27], v[224:227], v[64:67]\n"
    "ds_read_b64_tr_b16 v[20:21], v2 offset:20480\n"
    "v_mfma_f32_16x16x32_bf16 v[68:71], v[24:27], v[228:231], v[68:71]\n"
    "ds_read_b64_tr_b16 v[22:23], v2 offset:20992\n"
    "s_waitcnt lgkmcnt(4)\n"
    "v_mfma_f32_16x16x32_bf16 v[76:79], v[28:31], v[228:231], v[76:79]\n"
    "ds_read_b64_tr_b16 v[24:25], v1 offset:28672\n"
    "v_mfma_f32_16x16x32_bf16 v[72:75], v[28:31], v[224:227], v[72:75]\n"
    "ds_read_b64_tr_b16 v[26:27], v1 offset:29184\n"
    "s_waitcnt lgkmcnt(4)\n"
    "v_mfma_f32_16x16x32_bf16 v[112:115], v[16:19], v[232:235], v[112:115]\n"
    "ds_read_b64_tr_b16 v[28:29], v2 offset:28672\n"
    "v_mfma_f32_16x16x32_bf16 v[116:119], v[16:19], v[236:239], v[116:119]\n"
    "ds_read_b64_tr_b16 v[30:31], v2 offset:29184\n"
    "s_waitcnt lgkmcnt(4)\n"
    "v_mfma_f32_16x16x32_bf16 v[124:127], v[20:23], v[236:239], v[124:127]\n"
    "ds_read_b64_tr_b16 v[16:17], v1 offset:32768\n"
    "v_mfma_f32_16x16x32_bf16 v[120:123], v[20:23], v[232:235], v[120:123]\n"
    "ds_read_b64_tr_b16 v[18:19], v1 offset:33280\n"
    "s_waitcnt lgkmcnt(4)\n"
    "v_mfma_f32_16x16x32_bf16 v[64:67], v[24:27], v[232:235], v[64:67]\n"
    "ds_read_b64_tr_b16 v[20:21], v2 offset:32768\n"
    "v_mfma_f32_16x16x32_bf16 v[68:71], v[24:27], v[236:239], v[68:71]\n"
    "ds_read_b64_tr_b16 v[22:23], v2 offset:33280\n"
    "s_waitcnt lgkmcnt(4)\n"
    "v_mfma_f32_16x16x32_bf16 v[76:79], v[28:31], v[236:239], v[76:79]\n"
    "ds_read_b64_tr_b16 v[24:25], v1 offset:40960\n"
    "v_mfma_f32_16x16x32_bf16 v[72:75], v[28:31], v[232:235], v[72:75]\n"
    "ds_read_b64_tr_b16 v[26:27], v1 offset:41472\n"
    "s_cbranch_vccz .Lfz_RET_bY39\n"
    "s_barrier\n"
    ".Lfz_RET_bY39:\n"
    "s_add_u32 m0, s52, 0x10000\n"
    "s_mov_b32 s54, 0xa0000\n"
    "buffer_load_dwordx4 v9, s[36:39], s54 offen lds\n"
    "v_mov_b32 v14, 0\n"
    "s_mov_b64 exec, 1\n"
    "buffer_atomic_or v14, off, s[44:47], s6 offset:4 sc0\n"
    "s_mov_b64 exec, -1\n"
    "s_add_u32 m0, s52, 0x12000\n"
    "s_mov_b32 s55, 0xb0000\n"
    "buffer_load_dwordx4 v9, s[36:39], s55 offen lds\n"
    "s_mov_b32 s88, 0x6000\n"
    "buffer_load_dwordx4 v[152:155], v11, s[44:47], s88 offen offset:0 sc1\n"
    "s_mov_b32 s88, 0x6000\n"
    "buffer_load_dwordx4 v[156:159], v11, s[44:47], s88 offen offset:1024 sc1\n"
    "s_cbranch_vccnz .Lfz_RET_bY40\n"
    "s_barrier\n"
    ".Lfz_RET_bY40:\n"
    "s_waitcnt lgkmcnt(4)\n"
    "v_mfma_f32_16x16x32_bf16 v[112:115], v[16:19], v[240:243], v[112:115]\n"
    "ds_read_b64_tr_b16 v[28:29], v2 offset:40960\n"
    "v_mfma_f32_16x16x32_bf16 v[116:119], v[16:19], v[244:247], v[116:119]\n"
    "ds_read_b64_tr_b16 v[30:31], v2 offset:41472\n"
    "s_waitcnt lgkmcnt(4)\n"
    "v_mfma_f32_16x16x32_bf16 v[124:127], v[20:23], v[244:247], v[124:127]\n"
    "ds_read_b64_tr_b16 v[16:17], v1 offset:36864\n"
    "v_mfma_f32_16x16x32_bf16 v[120:123], v[20:23], v[240:243], v[120:123]\n"
    "ds_read_b64_tr_b16 v[18:19], v1 offset:37376\n"
    "s_waitcnt lgkmcnt(4)\n"
    "v_mfma_f32_16x16x32_bf16 v[64:67], v[24:27], v[240:243], v[64:67]\n"
    "ds_read_b64_tr_b16 v[20:21], v2 offset:36864\n"
    "v_mfma_f32_16x16x32_bf16 v[68:71], v[24:27], v[244:247], v[68:71]\n"
    "ds_read_b64_tr_b16 v[22:23], v2 offset:37376\n"
    "s_waitcnt lgkmcnt(4)\n"
    "v_mfma_f32_16x16x32_bf16 v[76:79], v[28:31], v[244:247], v[76:79]\n"
    "ds_read_b64_tr_b16 v[24:25], v1 offset:45056\n"
    "v_mfma_f32_16x16x32_bf16 v[72:75], v[28:31], v[240:243], v[72:75]\n"
    "ds_read_b64_tr_b16 v[26:27], v1 offset:45568\n"
    "s_waitcnt lgkmcnt(4)\n"
    "v_mfma_f32_16x16x32_bf16 v[112:115], v[16:19], v[248:251], v[112:115]\n"
    "ds_read_b64_tr_b16 v[28:29], v2 offset:45056\n"
    "v_mfma_f32_16x16x32_bf16 v[116:119], v[16:19], v[252:255], v[116:119]\n"
    "ds_read_b64_tr_b16 v[30:31], v2 offset:45568\n"
    "s_waitcnt lgkmcnt(4)\n"
    "v_mfma_f32_16x16x32_bf16 v[124:127], v[20:23], v[252:255], v[124:127]\n"
    "ds_read_b64_tr_b16 v[16:17], v6 offset:0\n"
    "v_mfma_f32_16x16x32_bf16 v[120:123], v[20:23], v[248:251], v[120:123]\n"
    "ds_read_b64_tr_b16 v[18:19], v6 offset:2048\n"
    "s_waitcnt lgkmcnt(4)\n"
    "v_mfma_f32_16x16x32_bf16 v[64:67], v[24:27], v[248:251], v[64:67]\n"
    "ds_read_b64_tr_b16 v[20:21], v7 offset:0\n"
    "v_mfma_f32_16x16x32_bf16 v[68:71], v[24:27], v[252:255], v[68:71]\n"
    "ds_read_b64_tr_b16 v[22:23], v7 offset:2048\n"
    "s_waitcnt lgkmcnt(4)\n"
    "v_mfma_f32_16x16x32_bf16 v[76:79], v[28:31], v[252:255], v[76:79]\n"
    "ds_read_b64_tr_b16 v[24:25], v8 offset:0\n"
    "v_mfma_f32_16x16x32_bf16 v[72:75], v[28:31], v[248:251], v[72:75]\n"
    "ds_read_b64_tr_b16 v[26:27], v8 offset:2048\n"
    "s_cbranch_vccz .Lfz_RET_bY41\n"
    "s_barrier\n"
    ".Lfz_RET_bY41:\n"
    "s_add_u32 m0, s52, 0x14000\n"
    "s_mov_b32 s54, 0xc0000\n"
    "buffer_load_dwordx4 v9, s[36:39], s54 offen lds\n"
    "s_waitcnt vmcnt(4)\n"
    "v_readfirstlane_b32 s90, v14\n"
    "s_sub_u32 s90, s90, s53\n"
    "s_cmp_eq_u32 s90, 4\n"
    "s_cbranch_scc0 .Lfz_SLOW_Y_2\n"
    ".Lfz_RET_Y_2:\n"
    "v_lshlrev_b32 v5, 3, v0\n"
    "v_sub_u32 v5, v11, v5\n"
    "v_mov_b32 v14, 0\n"
    "s_mov_b64 exec, 1\n"
    "buffer_atomic_or v14, off, s[44:47], s6 offset:8 sc0\n"
    "s_mov_b64 exec, -1\n"
    "v_mov_b32 v40, 0xbdd2d3e8\n"
    "v_mov_b32 v42, 0xc0135761\n"
    "v_mul_f32 v32, v112, v112\n"
    "v_mul_f32 v33, v113, v113\n"
    "v_mul_f32 v34, v114, v114\n"
    "v_mul_f32 v35, v115, v115\n"
    "v_fmaak_f32 v32, v40, v32, 0xc0135761\n"
    "v_fmaak_f32 v33, v40, v33, 0xc0135761\n"
    "v_fmaak_f32 v34, v40, v34, 0xc0135761\n"
    "v_fmaak_f32 v35, v40, v35, 0xc0135761\n"
    "v_mul_f32 v32, v32, v112\n"
    "v_mul_f32 v33, v33, v113\n"
    "v_mul_f32 v34, v34, v114\n"
    "v_mul_f32 v35, v35, v115\n"
    "v_exp_f32 v32, v32\n"
    "v_mul_f32 v64, v64, v112\n"
    "v_exp_f32 v33, v33\n"
    "s_add_u32 m0, s52, 0x16000\n"
    "s_mov_b32 s55, 0xd0000\n"
    "buffer_load_dwordx4 v9, s[36:39], s55 offen lds\n"
    "v_mul_f32 v65, v65, v113\n"
    "v_exp_f32 v34, v34\n"
    "v_mul_f32 v66, v66, v114\n"
    "v_exp_f32 v35, v35\n"
    "v_mul_f32 v67, v67, v115\n"
    "v_add_f32 v32, 1.0, v32\n"
    "v_add_f32 v33, 1.0, v33\n"
    "v_add_f32 v34, 1.0, v34\n"
    "v_add_f32 v35, 1.0, v35\n"
    "v_rcp_f32 v32, v32\n"
    "v_rcp_f32 v33, v33\n"
    "v_rcp_f32 v34, v34\n"
    "v_rcp_f32 v35, v35\n"
    "v_mul_f32 v32, v32, v64\n"
    "v_mul_f32 v33, v33, v65\n"
    "v_mul_f32 v34, v34, v66\n"
    "v_mul_f32 v35, v35, v67\n"
    "v_cvt_pk_bf16_f32 v36, v32, v33\n"
    "v_cvt_pk_bf16_f32 v37, v34, v35\n"
    "v_pk_mul_f32 v[32:33], v[120:121], v[120:121]\n"
    "s_mov_b32 s88, 0x18000\n"
    "buffer_load_dwordx2 v[160:161], v5, s[44:47], s88 offen offset:0 sc1\n"
    "buffer_load_dwordx2 v[162:163], v5, s[44:47], s88 offen offset:512 sc1\n"
    "v_pk_mul_f32 v[34:35], v[122:123], v[122:123]\n"
    "v_pk_mul_f32 v[72:73], v[72:73], v[120:121]\n"
    "v_pk_mul_f32 v[74:75], v[74:75], v[122:123]\n"
    "v_pk_fma_f32 v[32:33], v[32:33], v[40:41], v[42:43] op_sel_hi:[1,0,0]\n"
    "v_pk_fma_f32 v[34:35], v[34:35], v[40:41], v[42:43] op_sel_hi:[1,0,0]\n"
    "v_pk_mul_f32 v[32:33], v[32:33], v[120:121]\n"
    "v_pk_mul_f32 v[34:35], v[34:35], v[122:123]\n"
    "v_exp_f32 v32, v32\n"
    "v_exp_f32 v33, v33\n"
    "v_exp_f32 v34, v34\n"
    "v_exp_f32 v35, v35\n"
    "v_pk_add_f32 v[32:33], v[32:33], 1.0 op_sel_hi:[1,0]\n"
    "v_pk_add_f32 v[34:35], v[34:35], 1.0 op_sel_hi:[1,0]\n"
    "v_rcp_f32 v32, v32\n"
    "v_rcp_f32 v33, v33\n"
    "v_rcp_f32 v34, v34\n"
    "v_rcp_f32 v35, v35\n"
    "s_mov_b32 s88, 0x18000\n"
    "buffer_load_dwordx2 v[164:165], v5, s[44:47], s88 offen offset:1024 sc1\n"
    "buffer_load_dwordx2 v[166:167], v5, s[44:47], s88 offen offset:1536 sc1\n"
    "v_pk_mul_f32 v[32:33], v[32:33], v[72:73]\n"
    "v_pk_mul_f32 v[34:35], v[34:35], v[74:75]\n"
    "v_cvt_pk_bf16_f32 v38, v32, v33\n"
    "v_cvt_pk_bf16_f32 v39, v34, v35\n"
    "buffer_store_dwordx4 v[36:39], v10, s[44:47], s69 offen offset:0 sc1\n"
    "v_mul_f32 v32, v116, v116\n"
    "v_mul_f32 v33, v117, v117\n"
    "v_mul_f32 v34, v118, v118\n"
    "v_mul_f32 v35, v119, v119\n"
    "v_fmaak_f32 v32, v40, v32, 0xc0135761\n"
    "v_fmaak_f32 v33, v40, v33, 0xc0135761\n"
    "v_fmaak_f32 v34, v40, v34, 0xc0135761\n"
    "v_fmaak_f32 v35, v40, v35, 0xc0135761\n"
    "v_mul_f32 v32, v32, v116\n"
    "v_mul_f32 v33, v33, v117\n"
    "v_mul_f32 v34, v34, v118\n"
    "v_mul_f32 v35, v35, v119\n"
    "v_exp_f32 v32, v32\n"
    "v_mul_f32 v68, v68, v116\n"
    "v_exp_f32 v33, v33\n"
    "v_mul_f32 v69, v69, v117\n"
    "v_exp_f32 v34, v34\n"
    "v_mul_f32 v70, v70, v118\n"
    "v_exp_f32 v35, v35\n"
    "s_mov_b32 s88, 0x1a000\n"
    "buffer_load_dwordx2 v[168:169], v5, s[44:47], s88 offen offset:0 sc1\n"
    "buffer_load_dwordx2 v[170:171], v5, s[44:47], s88 offen offset:512 sc1\n"
    "v_mul_f32 v71, v71, v119\n"
    "v_add_f32 v32, 1.0, v32\n"
    "v_add_f32 v33, 1.0, v33\n"
    "v_add_f32 v34, 1.0, v34\n"
    "v_add_f32 v35, 1.0, v35\n"
    "v_rcp_f32 v32, v32\n"
    "v_rcp_f32 v33, v33\n"
    "v_rcp_f32 v34, v34\n"
    "v_rcp_f32 v35, v35\n"
    "v_mul_f32 v32, v32, v68\n"
    "v_mul_f32 v33, v33, v69\n"
    "v_mul_f32 v34, v34, v70\n"
    "v_mul_f32 v35, v35, v71\n"
    "v_cvt_pk_bf16_f32 v36, v32, v33\n"
    "v_cvt_pk_bf16_f32 v37, v34, v35\n"
    "v_pk_mul_f32 v[32:33], v[124:125], v[124:125]\n"
    "v_pk_mul_f32 v[34:35], v[126:127], v[126:127]\n"
    "v_pk_mul_f32 v[76:77], v[76:77], v[124:125]\n"
    "v_pk_mul_f32 v[78:79], v[78:79], v[126:127]\n"
    "v_pk_fma_f32 v[32:33], v[32:33], v[40:41], v[42:43] op_sel_hi:[1,0,0]\n"
    "s_mov_b32 s88, 0x1a000\n"
    "buffer_load_dwordx2 v[172:173], v5, s[44:47], s88 offen offset:1024 sc1\n"
    "buffer_load_dwordx2 v[174:175], v5, s[44:47], s88 offen offset:1536 sc1\n"
    "v_pk_fma_f32 v[34:35], v[34:35], v[40:41], v[42:43] op_sel_hi:[1,0,0]\n"
    "v_pk_mul_f32 v[32:33], v[32:33], v[124:125]\n"
    "v_pk_mul_f32 v[34:35], v[34:35], v[126:127]\n"
    "v_exp_f32 v32, v32\n"
    "v_exp_f32 v33, v33\n"
    "v_exp_f32 v34, v34\n"
    "v_exp_f32 v35, v35\n"
    "v_pk_add_f32 v[32:33], v[32:33], 1.0 op_sel_hi:[1,0]\n"
    "v_pk_add_f32 v[34:35], v[34:35], 1.0 op_sel_hi:[1,0]\n"
    "v_rcp_f32 v32, v32\n"
    "v_rcp_f32 v33, v33\n"
    "v_rcp_f32 v34, v34\n"
    "v_rcp_f32 v35, v35\n"
    "v_pk_mul_f32 v[32:33], v[32:33], v[76:77]\n"
    "v_pk_mul_f32 v[34:35], v[34:35], v[78:79]\n"
    "v_cvt_pk_bf16_f32 v38, v32, v33\n"
    "v_cvt_pk_bf16_f32 v39, v34, v35\n"
    "buffer_store_dwordx4 v[36:39], v10, s[44:47], s69 offen offset:1024 sc1\n"
    "s_cbranch_vccnz .Lfz_RET_bY42\n"
    "s_barrier\n"
    ".Lfz_RET_bY42:\n"
    "s_waitcnt lgkmcnt(4)\n"
    "v_mfma_f32_16x16x32_bf16 v[96:99], v[16:19], v[128:131], 0\n"
    "ds_read_b64_tr_b16 v[28:29], v4 offset:0\n"
    "v_mfma_f32_16x16x32_bf16 v[100:103], v[16:19], v[132:135], 0\n"
    "ds_read_b64_tr_b16 v[30:31], v4 offset:2048\n"
    "s_waitcnt lgkmcnt(4)\n"
    "v_mfma_f32_16x16x32_bf16 v[108:111], v[20:23], v[132:135], 0\n"
    "ds_read_b64_tr_b16 v[16:17], v6 offset:16384\n"
    "v_mfma_f32_16x16x32_bf16 v[104:107], v[20:23], v[128:131], 0\n"
    "ds_read_b64_tr_b16 v[18:19], v6 offset:18432\n"
    "s_waitcnt lgkmcnt(4)\n"
    "v_mfma_f32_16x16x32_bf16 v[80:83], v[24:27], v[128:131], 0\n"
    "ds_read_b64_tr_b16 v[20:21], v7 offset:16384\n"
    "v_mfma_f32_16x16x32_bf16 v[84:87], v[24:27], v[132:135], 0\n"
    "ds_read_b64_tr_b16 v[22:23], v7 offset:18432\n"
    "s_waitcnt lgkmcnt(4)\n"
    "v_mfma_f32_16x16x32_bf16 v[92:95], v[28:31], v[132:135], 0\n"
    "ds_read_b64_tr_b16 v[24:25], v8 offset:16384\n"
    "v_mfma_f32_16x16x32_bf16 v[88:91], v[28:31], v[128:131], 0\n"
    "ds_read_b64_tr_b16 v[26:27], v8 offset:18432\n"
    "s_waitcnt lgkmcnt(4)\n"
    "v_mfma_f32_16x16x32_bf16 v[96:99], v[16:19], v[136:139], v[96:99]\n"
    "ds_read_b64_tr_b16 v[28:29], v4 offset:16384\n"
    "v_mfma_f32_16x16x32_bf16 v[100:103], v[16:19], v[140:143], v[100:103]\n"
    "ds_read_b64_tr_b16 v[30:31], v4 offset:18432\n"
    "s_waitcnt lgkmcnt(4)\n"
    "v_mfma_f32_16x16x32_bf16 v[108:111], v[20:23], v[140:143], v[108:111]\n"
    "ds_read_b64_tr_b16 v[16:17], v6 offset:32768\n"
    "v_mfma_f32_16x16x32_bf16 v[104:107], v[20:23], v[136:139], v[104:107]\n"
    "ds_read_b64_tr_b16 v[18:19], v6 offset:34816\n"
    "s_waitcnt lgkmcnt(4)\n"
    "v_mfma_f32_16x16x32_bf16 v[80:83], v[24:27], v[136:139], v[80:83]\n"
    "ds_read_b64_tr_b16 v[20:21], v7 offset:32768\n"
    "v_mfma_f32_16x16x32_bf16 v[84:87], v[24:27], v[140:143], v[84:87]\n"
    "ds_read_b64_tr_b16 v[22:23], v7 offset:34816\n"
    "s_waitcnt lgkmcnt(4)\n"
    "v_mfma_f32_16x16x32_bf16 v[92:95], v[28:31], v[140:143], v[92:95]\n"
    "ds_read_b64_tr_b16 v[24:25], v8 offset:32768\n"
    "v_mfma_f32_16x16x32_bf16 v[88:91], v[28:31], v[136:139], v[88:91]\n"
    "ds_read_b64_tr_b16 v[26:27], v8 offset:34816\n"
    "s_cbranch_vccz .Lfz_RET_bY43\n"
    "s_barrier\n"
    ".Lfz_RET_bY43:\n"
    "s_add_u32 m0, s52, 0x18000\n"
    "s_mov_b32 s54, 0xe0000\n"
    "buffer_load_dwordx4 v9, s[36:39], s54 offen lds\n"
    "s_waitcnt vmcnt(12)\n"
    "v_readfirstlane_b32 s90, v14\n"
    "s_sub_u32 s90, s90, s53\n"
    "s_cmp_eq_u32 s90, 4\n"
    "s_cbranch_scc0 .Lfz_SLOW_Y_3\n"
    ".Lfz_RET_Y_3:\n"
    "s_add_u32 m0, s52, 0x1a000\n"
    "s_mov_b32 s55, 0xf0000\n"
    "buffer_load_dwordx4 v9, s[36:39], s55 offen lds\n"
    "s_mov_b32 s88, 0x8000\n"
    "buffer_load_dwordx4 v[176:179], v11, s[44:47], s88 offen offset:0 sc1\n"
    "s_mov_b32 s88, 0x8000\n"
    "buffer_load_dwordx4 v[180:183], v11, s[44:47], s88 offen offset:1024 sc1\n"
    "s_mov_b32 s88, 0xa000\n"
    "buffer_load_dwordx4 v[184:187], v11, s[44:47], s88 offen offset:0 sc1\n"
    "s_mov_b32 s88, 0xa000\n"
    "buffer_load_dwordx4 v[188:191], v11, s[44:47], s88 offen offset:1024 sc1\n"
    "s_cbranch_vccnz .Lfz_RET_bY44\n"
    "s_barrier\n"
    ".Lfz_RET_bY44:\n"
    "s_waitcnt lgkmcnt(4)\n"
    "v_mfma_f32_16x16x32_bf16 v[96:99], v[16:19], v[144:147], v[96:99]\n"
    "ds_read_b64_tr_b16 v[28:29], v4 offset:32768\n"
    "v_mfma_f32_16x16x32_bf16 v[100:103], v[16:19], v[148:151], v[100:103]\n"
    "ds_read_b64_tr_b16 v[30:31], v4 offset:34816\n"
    "s_waitcnt lgkmcnt(4)\n"
    "v_mfma_f32_16x16x32_bf16 v[108:111], v[20:23], v[148:151], v[108:111]\n"
    "ds_read_b64_tr_b16 v[16:17], v6 offset:49152\n"
    "v_mfma_f32_16x16x32_bf16 v[104:107], v[20:23], v[144:147], v[104:107]\n"
    "ds_read_b64_tr_b16 v[18:19], v6 offset:51200\n"
    "s_waitcnt lgkmcnt(4)\n"
    "v_mfma_f32_16x16x32_bf16 v[80:83], v[24:27], v[144:147], v[80:83]\n"
    "ds_read_b64_tr_b16 v[20:21], v7 offset:49152\n"
    "v_mfma_f32_16x16x32_bf16 v[84:87], v[24:27], v[148:151], v[84:87]\n"
    "ds_read_b64_tr_b16 v[22:23], v7 offset:51200\n"
    "s_waitcnt lgkmcnt(4)\n"
    "v_mfma_f32_16x16x32_bf16 v[92:95], v[28:31], v[148:151], v[92:95]\n"
    "ds_read_b64_tr_b16 v[24:25], v8 offset:49152\n"
    "v_mfma_f32_16x16x32_bf16 v[88:91], v[28:31], v[144:147], v[88:91]\n"
    "ds_read_b64_tr_b16 v[26:27], v8 offset:51200\n"
    "s_waitcnt lgkmcnt(4)\n"
    "v_mfma_f32_16x16x32_bf16 v[96:99], v[16:19], v[152:155], v[96:99]\n"
    "ds_read_b64_tr_b16 v[28:29], v4 offset:49152\n"
    "v_mfma_f32_16x16x32_bf16 v[100:103], v[16:19], v[156:159], v[100:103]\n"
    "ds_read_b64_tr_b16 v[30:31], v4 offset:51200\n"
    "s_waitcnt lgkmcnt(4)\n"
    "v_mfma_f32_16x16x32_bf16 v[108:111], v[20:23], v[156:159], v[108:111]\n"
    "v_add_u32 v6, 0x1c000, v6\n"
    "ds_read_b64_tr_b16 v[16:17], v6 offset:0\n"
    "v_mfma_f32_16x16x32_bf16 v[104:107], v[20:23], v[152:155], v[104:107]\n"
    "ds_read_b64_tr_b16 v[18:19], v6 offset:2048\n"
    "s_waitcnt lgkmcnt(4)\n"
    "v_mfma_f32_16x16x32_bf16 v[80:83], v[24:27], v[152:155], v[80:83]\n"
    "v_add_u32 v7, 0x1c000, v7\n"
    "ds_read_b64_tr_b16 v[20:21], v7 offset:0\n"
    "v_mfma_f32_16x16x32_bf16 v[84:87], v[24:27], v[156:159], v[84:87]\n"
    "ds_read_b64_tr_b16 v[22:23], v7 offset:2048\n"
    "s_waitcnt lgkmcnt(4)\n"
    "v_mfma_f32_16x16x32_bf16 v[92:95], v[28:31], v[156:159], v[92:95]\n"
    "v_add_u32 v8, 0x1c000, v8\n"
    "ds_read_b64_tr_b16 v[24:25], v8 offset:0\n"
    "v_mfma_f32_16x16x32_bf16 v[88:91], v[28:31], v[152:155], v[88:91]\n"
    "ds_read_b64_tr_b16 v[26:27], v8 offset:2048\n"
    "s_cbranch_vccz .Lfz_RET_bY45\n"
    "s_barrier\n"
    ".Lfz_RET_bY45:\n"
    "s_add_u32 m0, s52, 0x0\n"
    "s_mov_b32 s54, 0x100000\n"
    "buffer_load_dwordx4 v9, s[36:39], s54 offen lds\n"
    "s_waitcnt vmcnt(7)\n"
    "s_mov_b64 exec, 1\n"
    "buffer_atomic_add v12, off, s[44:47], s64 offset:12\n"
    "s_mov_b64 exec, -1\n"
    "s_add_u32 m0, s52, 0x2000\n"
    "s_mov_b32 s55, 0x110000\n"
    "buffer_load_dwordx4 v9, s[36:39], s55 offen lds\n"
    "s_add_u32 m0, s52, 0x4000\n"
    "s_mov_b32 s54, 0x120000\n"
    "buffer_load_dwordx4 v9, s[36:39], s54 offen lds\n"
    "s_add_u32 m0, s52, 0x6000\n"
    "s_mov_b32 s55, 0x130000\n"
    "buffer_load_dwordx4 v9, s[36:39], s55 offen lds\n"
    "s_mov_b32 s88, 0xc000\n"
    "buffer_load_dwordx4 v[192:195], v11, s[44:47], s88 offen offset:0 sc1\n"
    "s_mov_b32 s88, 0xc000\n"
    "buffer_load_dwordx4 v[196:199], v11, s[44:47], s88 offen offset:1024 sc1\n"
    "s_mov_b32 s88, 0xe000\n"
    "buffer_load_dwordx4 v[200:203], v11, s[44:47], s88 offen offset:0 sc1\n"
    "s_mov_b32 s88, 0xe000\n"
    "buffer_load_dwordx4 v[204:207], v11, s[44:47], s88 offen offset:1024 sc1\n"
    "s_cbranch_vccnz .Lfz_RET_bY46\n"
    "s_barrier\n"
    ".Lfz_RET_bY46:\n"
    "s_waitcnt lgkmcnt(4)\n"
    "v_mfma_f32_16x16x32_bf16 v[96:99], v[16:19], v[160:163], v[96:99]\n"
    "v_add_u32 v4, 0x1c000, v4\n"
    "ds_read_b64_tr_b16 v[28:29], v4 offset:0\n"
    "v_mfma_f32_16x16x32_bf16 v[100:103], v[16:19], v[164:167], v[100:103]\n"
    "ds_read_b64_tr_b16 v[30:31], v4 offset:2048\n"
    "s_waitcnt lgkmcnt(4)\n"
    "v_mfma_f32_16x16x32_bf16 v[108:111], v[20:23], v[164:167], v[108:111]\n"
    "ds_read_b64_tr_b16 v[16:17], v6 offset:16384\n"
    "v_mfma_f32_16x16x32_bf16 v[104:107], v[20:23], v[160:163], v[104:107]\n"
    "ds_read_b64_tr_b16 v[18:19], v6 offset:18432\n"
    "s_waitcnt lgkmcnt(4)\n"
    "v_mfma_f32_16x16x32_bf16 v[80:83], v[24:27], v[160:163], v[80:83]\n"
    "ds_read_b64_tr_b16 v[20:21], v7 offset:16384\n"
    "v_mfma_f32_16x16x32_bf16 v[84:87], v[24:27], v[164:167], v[84:87]\n"
    "ds_read_b64_tr_b16 v[22:23], v7 offset:18432\n"
    "s_waitcnt lgkmcnt(4)\n"
    "v_mfma_f32_16x16x32_bf16 v[92:95], v[28:31], v[164:167], v[92:95]\n"
    "ds_read_b64_tr_b16 v[24:25], v8 offset:16384\n"
    "v_mfma_f32_16x16x32_bf16 v[88:91], v[28:31], v[160:163], v[88:91]\n"
    "ds_read_b64_tr_b16 v[26:27], v8 offset:18432\n"
    "s_waitcnt lgkmcnt(4)\n"
    "v_mfma_f32_16x16x32_bf16 v[96:99], v[16:19], v[168:171], v[96:99]\n"
    "ds_read_b64_tr_b16 v[28:29], v4 offset:16384\n"
    "v_mfma_f32_16x16x32_bf16 v[100:103], v[16:19], v[172:175], v[100:103]\n"
    "ds_read_b64_tr_b16 v[30:31], v4 offset:18432\n"
    "s_waitcnt lgkmcnt(4)\n"
    "v_mfma_f32_16x16x32_bf16 v[108:111], v[20:23], v[172:175], v[108:111]\n"
    "ds_read_b64_tr_b16 v[16:17], v6 offset:32768\n"
    "v_mfma_f32_16x16x32_bf16 v[104:107], v[20:23], v[168:171], v[104:107]\n"
    "ds_read_b64_tr_b16 v[18:19], v6 offset:34816\n"
    "s_waitcnt lgkmcnt(4)\n"
    "v_mfma_f32_16x16x32_bf16 v[80:83], v[24:27], v[168:171], v[80:83]\n"
    "ds_read_b64_tr_b16 v[20:21], v7 offset:32768\n"
    "v_mfma_f32_16x16x32_bf16 v[84:87], v[24:27], v[172:175], v[84:87]\n"
    "ds_read_b64_tr_b16 v[22:23], v7 offset:34816\n"
    "s_waitcnt lgkmcnt(4)\n"
    "v_mfma_f32_16x16x32_bf16 v[92:95], v[28:31], v[172:175], v[92:95]\n"
    "ds_read_b64_tr_b16 v[24:25], v8 offset:32768\n"
    "v_mfma_f32_16x16x32_bf16 v[88:91], v[28:31], v[168:171], v[88:91]\n"
    "ds_read_b64_tr_b16 v[26:27], v8 offset:34816\n"
    "s_cbranch_vccz .Lfz_RET_bY47\n"
    "s_barrier\n"
    ".Lfz_RET_bY47:\n"
    "s_add_u32 m0, s52, 0x8000\n"
    "s_mov_b32 s54, 0x140000\n"
    "buffer_load_dwordx4 v9, s[36:39], s54 offen lds\n"
    "v_mov_b32 v14, 0\n"
    "s_mov_b64 exec, 1\n"
    "buffer_atomic_or v14, off, s[44:47], s6 offset:12 sc0\n"
    "s_mov_b64 exec, -1\n"
    "s_add_u32 m0, s52, 0xa000\n"
    "s_mov_b32 s55, 0x150000\n"
    "buffer_load_dwordx4 v9, s[36:39], s55 offen lds\n"
    "s_add_u32 m0, s52, 0xc000\n"
    "s_mov_b32 s54, 0x160000\n"
    "buffer_load_dwordx4 v9, s[36:39], s54 offen lds\n"
    "s_add_u32 m0, s52, 0xe000\n"
    "s_mov_b32 s55, 0x170000\n"
    "buffer_load_dwordx4 v9, s[36:39], s55 offen lds\n"
    "s_cbranch_vccnz .Lfz_RET_bY48\n"
    "s_barrier\n"
    ".Lfz_RET_bY48:\n"
    "s_waitcnt vmcnt(17) lgkmcnt(4)\n"
    "v_mfma_f32_16x16x32_bf16 v[96:99], v[16:19], v[176:179], v[96:99]\n"
    "ds_read_b64_tr_b16 v[28:29], v4 offset:32768\n"
    "s_waitcnt vmcnt(16)\n"
    "v_mfma_f32_16x16x32_bf16 v[100:103], v[16:19], v[180:183], v[100:103]\n"
    "ds_read_b64_tr_b16 v[30:31], v4 offset:34816\n"
    "s_waitcnt lgkmcnt(4)\n"
    "v_mfma_f32_16x16x32_bf16 v[108:111], v[20:23], v[180:183], v[108:111]\n"
    "v_add_u32 v6, 0xffff4000, v6\n"
    "ds_read_b64_tr_b16 v[16:17], v6 offset:0\n"
    "v_mfma_f32_16x16x32_bf16 v[104:107], v[20:23], v[176:179], v[104:107]\n"
    "ds_read_b64_tr_b16 v[18:19], v6 offset:2048\n"
    "s_waitcnt lgkmcnt(4)\n"
    "v_mfma_f32_16x16x32_bf16 v[80:83], v[24:27], v[176:179], v[80:83]\n"
    "v_add_u32 v7, 0xffff4000, v7\n"
    "ds_read_b64_tr_b16 v[20:21], v7 offset:0\n"
    "v_mfma_f32_16x16x32_bf16 v[84:87], v[24:27], v[180:183], v[84:87]\n"
    "ds_read_b64_tr_b16 v[22:23], v7 offset:2048\n"
    "s_waitcnt lgkmcnt(4)\n"
    "v_mfma_f32_16x16x32_bf16 v[92:95], v[28:31], v[180:183], v[92:95]\n"
    "v_add_u32 v8, 0xffff4000, v8\n"
    "ds_read_b64_tr_b16 v[24:25], v8 offset:0\n"
    "v_mfma_f32_16x16x32_bf16 v[88:91], v[28:31], v[176:179], v[88:91]\n"
    "ds_read_b64_tr_b16 v[26:27], v8 offset:2048\n"
    "s_waitcnt vmcnt(15) lgkmcnt(4)\n"
    "v_mfma_f32_16x16x32_bf16 v[96:99], v[16:19], v[184:187], v[96:99]\n"
    "v_add_u32 v4, 0xffff4000, v4\n"
    "ds_read_b64_tr_b16 v[28:29], v4 offset:0\n"
    "s_waitcnt vmcnt(14)\n"
    "v_mfma_f32_16x16x32_bf16 v[100:103], v[16:19], v[188:191], v[100:103]\n"
    "ds_read_b64_tr_b16 v[30:31], v4 offset:2048\n"
    "s_waitcnt lgkmcnt(4)\n"
    "v_mfma_f32_16x16x32_bf16 v[108:111], v[20:23], v[188:191], v[108:111]\n"
    "ds_read_b64_tr_b16 v[16:17], v6 offset:16384\n"
    "v_mfma_f32_16x16x32_bf16 v[104:107], v[20:23], v[184:187], v[104:107]\n"
    "ds_read_b64_tr_b16 v[18:19], v6 offset:18432\n"
    "s_waitcnt lgkmcnt(4)\n"
    "v_mfma_f32_16x16x32_bf16 v[80:83], v[24:27], v[184:187], v[80:83]\n"
    "ds_read_b64_tr_b16 v[20:21], v7 offset:16384\n"
    "v_mfma_f32_16x16x32_bf16 v[84:87], v[24:27], v[188:191], v[84:87]\n"
    "ds_read_b64_tr_b16 v[22:23], v7 offset:18432\n"
    "s_waitcnt lgkmcnt(4)\n"
    "v_mfma_f32_16x16x32_bf16 v[92:95], v[28:31], v[188:191], v[92:95]\n"
    "ds_read_b64_tr_b16 v[24:25], v8 offset:16384\n"
    "v_mfma_f32_16x16x32_bf16 v[88:91], v[28:31], v[184:187], v[88:91]\n"
    "ds_read_b64_tr_b16 v[26:27], v8 offset:18432\n"
    "s_waitcnt vmcnt(11)\n"
    "s_cbranch_vccz .Lfz_RET_bY49\n"
    "s_barrier\n"
    ".Lfz_RET_bY49:\n"
    "s_add_u32 m0, s52, 0x1c000\n"
    "s_mov_b32 s54, 0x0\n"
    "buffer_load_dwordx4 v9, s[24:27], s54 offen lds\n"
    "s_waitcnt vmcnt(4)\n"
    "v_readfirstlane_b32 s90, v14\n"
    "s_sub_u32 s90, s90, s53\n"
    "s_cmp_eq_u32 s90, 4\n"
    "s_cbranch_scc0 .Lfz_SLOW_Y_4\n"
    ".Lfz_RET_Y_4:\n"
    "s_add_u32 m0, s52, 0x1e000\n"
    "s_mov_b32 s55, 0x10000\n"
    "buffer_load_dwordx4 v9, s[24:27], s55 offen lds\n"
    "s_add_u32 m0, s52, 0x20000\n"
    "s_mov_b32 s54, 0x20000\n"
    "buffer_load_dwordx4 v9, s[24:27], s54 offen lds\n"
    "s_add_u32 m0, s52, 0x22000\n"
    "s_mov_b32 s55, 0x30000\n"
    "buffer_load_dwordx4 v9, s[24:27], s55 offen lds\n"
    "s_mov_b32 s88, 0x10000\n"
    "buffer_load_dwordx4 v[208:211], v11, s[44:47], s88 offen offset:0 sc1\n"
    "s_mov_b32 s88, 0x10000\n"
    "buffer_load_dwordx4 v[212:215], v11, s[44:47], s88 offen offset:1024 sc1\n"
    "s_mov_b32 s88, 0x12000\n"
    "buffer_load_dwordx4 v[216:219], v11, s[44:47], s88 offen offset:0 sc1\n"
    "s_mov_b32 s88, 0x12000\n"
    "buffer_load_dwordx4 v[220:223], v11, s[44:47], s88 offen offset:1024 sc1\n"
    "s_mov_b32 s88, 0x14000\n"
    "buffer_load_dwordx4 v[224:227], v11, s[44:47], s88 offen offset:0 sc1\n"
    "s_mov_b32 s88, 0x14000\n"
    "buffer_load_dwordx4 v[228:231], v11, s[44:47], s88 offen offset:1024 sc1\n"
    "s_mov_b32 s88, 0x16000\n"
    "buffer_load_dwordx4 v[232:235], v11, s[44:47], s88 offen offset:0 sc1\n"
    "s_mov_b32 s88, 0x16000\n"
    "buffer_load_dwordx4 v[236:239], v11, s[44:47], s88 offen offset:1024 sc1\n"
    "s_cbranch_vccnz .Lfz_RET_bY50\n"
    "s_barrier\n"
    ".Lfz_RET_bY50:\n"
    "s_waitcnt lgkmcnt(4)\n"
    "v_mfma_f32_16x16x32_bf16 v[96:99], v[16:19], v[192:195], v[96:99]\n"
    "ds_read_b64_tr_b16 v[28:29], v4 offset:16384\n"
    "v_mfma_f32_16x16x32_bf16 v[100:103], v[16:19], v[196:199], v[100:103]\n"
    "ds_read_b64_tr_b16 v[30:31], v4 offset:18432\n"
    "s_waitcnt lgkmcnt(4)\n"
    "v_mfma_f32_16x16x32_bf16 v[108:111], v[20:23], v[196:199], v[108:111]\n"
    "ds_read_b64_tr_b16 v[16:17], v6 offset:32768\n"
    "v_mfma_f32_16x16x32_bf16 v[104:107], v[20:23], v[192:195], v[104:107]\n"
    "ds_read_b64_tr_b16 v[18:19], v6 offset:34816\n"
    "s_waitcnt lgkmcnt(4)\n"
    "v_mfma_f32_16x16x32_bf16 v[80:83], v[24:27], v[192:195], v[80:83]\n"
    "ds_read_b64_tr_b16 v[20:21], v7 offset:32768\n"
    "v_mfma_f32_16x16x32_bf16 v[84:87], v[24:27], v[196:199], v[84:87]\n"
    "ds_read_b64_tr_b16 v[22:23], v7 offset:34816\n"
    "s_waitcnt lgkmcnt(4)\n"
    "v_mfma_f32_16x16x32_bf16 v[92:95], v[28:31], v[196:199], v[92:95]\n"
    "ds_read_b64_tr_b16 v[24:25], v8 offset:32768\n"
    "v_mfma_f32_16x16x32_bf16 v[88:91], v[28:31], v[192:195], v[88:91]\n"
    "ds_read_b64_tr_b16 v[26:27], v8 offset:34816\n"
    "s_waitcnt lgkmcnt(4)\n"
    "v_mfma_f32_16x16x32_bf16 v[96:99], v[16:19], v[200:203], v[96:99]\n"
    "ds_read_b64_tr_b16 v[28:29], v4 offset:32768\n"
    "v_mfma_f32_16x16x32_bf16 v[100:103], v[16:19], v[204:207], v[100:103]\n"
    "ds_read_b64_tr_b16 v[30:31], v4 offset:34816\n"
    "s_waitcnt lgkmcnt(4)\n"
    "v_mfma_f32_16x16x32_bf16 v[108:111], v[20:23], v[204:207], v[108:111]\n"
    "v_add_u32 v6, 0xffff0000, v6\n"
    "ds_read_b64_tr_b16 v[16:17], v6 offset:0\n"
    "v_mfma_f32_16x16x32_bf16 v[104:107], v[20:23], v[200:203], v[104:107]\n"
    "ds_read_b64_tr_b16 v[18:19], v6 offset:2048\n"
    "s_waitcnt lgkmcnt(4)\n"
    "v_mfma_f32_16x16x32_bf16 v[80:83], v[24:27], v[200:203], v[80:83]\n"
    "v_add_u32 v7, 0xffff0000, v7\n"
    "ds_read_b64_tr_b16 v[20:21], v7 offset:0\n"
    "v_mfma_f32_16x16x32_bf16 v[84:87], v[24:27], v[204:207], v[84:87]\n"
    "ds_read_b64_tr_b16 v[22:23], v7 offset:2048\n"
    "s_waitcnt lgkmcnt(4)\n"
    "v_mfma_f32_16x16x32_bf16 v[92:95], v[28:31], v[204:207], v[92:95]\n"
    "v_add_u32 v8, 0xffff0000, v8\n"
    "ds_read_b64_tr_b16 v[24:25], v8 offset:0\n"
    "v_mfma_f32_16x16x32_bf16 v[88:91], v[28:31], v[200:203], v[88:91]\n"
    "ds_read_b64_tr_b16 v[26:27], v8 offset:2048\n"
    "s_waitcnt vmcnt(14)\n"
    "s_cbranch_vccz .Lfz_RET_bY51\n"
    "s_barrier\n"
    ".Lfz_RET_bY51:\n"
    "s_add_u32 m0, s52, 0x24000\n"
    "s_mov_b32 s54, 0x40000\n"
    "buffer_load_dwordx4 v9, s[24:27], s54 offen lds\n"
    "s_add_u32 m0, s52, 0x26000\n"
    "s_mov_b32 s55, 0x50000\n"
    "buffer_load_dwordx4 v9, s[24:27], s55 offen lds\n"
    "s_add_u32 m0, s52, 0x10000\n"
    "s_mov_b32 s54, 0x60000\n"
    "buffer_load_dwordx4 v9, s[24:27], s54 offen lds\n"
    "s_add_u32 m0, s52, 0x12000\n"
    "s_mov_b32 s55, 0x70000\n"
    "buffer_load_dwordx4 v9, s[24:27], s55 offen lds\n"
    "s_cbranch_vccnz .Lfz_RET_bY52\n"
    "s_barrier\n"
    ".Lfz_RET_bY52:\n"
    "s_waitcnt vmcnt(11) lgkmcnt(4)\n"
    "v_mfma_f32_16x16x32_bf16 v[96:99], v[16:19], v[208:211], v[96:99]\n"
    "v_add_u32 v4, 0xffff0000, v4\n"
    "ds_read_b64_tr_b16 v[28:29], v4 offset:0\n"
    "s_waitcnt vmcnt(10)\n"
    "v_mfma_f32_16x16x32_bf16 v[100:103], v[16:19], v[212:215], v[100:103]\n"
    "ds_read_b64_tr_b16 v[30:31], v4 offset:2048\n"
    "s_waitcnt lgkmcnt(4)\n"
    "v_mfma_f32_16x16x32_bf16 v[108:111], v[20:23], v[212:215], v[108:111]\n"
    "ds_read_b64_tr_b16 v[16:17], v6 offset:16384\n"
    "v_mfma_f32_16x16x32_bf16 v[104:107], v[20:23], v[208:211], v[104:107]\n"
    "ds_read_b64_tr_b16 v[18:19], v6 offset:18432\n"
    "s_waitcnt lgkmcnt(4)\n"
    "v_mfma_f32_16x16x32_bf16 v[80:83], v[24:27], v[208:211], v[80:83]\n"
    "ds_read_b64_tr_b16 v[20:21], v7 offset:16384\n"
    "v_mfma_f32_16x16x32_bf16 v[84:87], v[24:27], v[212:215], v[84:87]\n"
    "ds_read_b64_tr_b16 v[22:23], v7 offset:18432\n"
    "s_waitcnt lgkmcnt(4)\n"
    "v_mfma_f32_16x16x32_bf16 v[92:95], v[28:31], v[212:215], v[92:95]\n"
    "ds_read_b64_tr_b16 v[24:25], v8 offset:16384\n"
    "v_mfma_f32_16x16x32_bf16 v[88:91], v[28:31], v[208:211], v[88:91]\n"
    "ds_read_b64_tr_b16 v[26:27], v8 offset:18432\n"
    "s_waitcnt vmcnt(9) lgkmcnt(4)\n"
    "v_mfma_f32_16x16x32_bf16 v[96:99], v[16:19], v[216:219], v[96:99]\n"
    "ds_read_b64_tr_b16 v[28:29], v4 offset:16384\n"
    "s_waitcnt vmcnt(8)\n"
    "v_mfma_f32_16x16x32_bf16 v[100:103], v[16:19], v[220:223], v[100:103]\n"
    "ds_read_b64_tr_b16 v[30:31], v4 offset:18432\n"
    "s_waitcnt lgkmcnt(4)\n"
    "v_mfma_f32_16x16x32_bf16 v[108:111], v[20:23], v[220:223], v[108:111]\n"
    "ds_read_b64_tr_b16 v[16:17], v6 offset:32768\n"
    "v_mfma_f32_16x16x32_bf16 v[104:107], v[20:23], v[216:219], v[104:107]\n"
    "ds_read_b64_tr_b16 v[18:19], v6 offset:34816\n"
    "s_waitcnt lgkmcnt(4)\n"
    "v_mfma_f32_16x16x32_bf16 v[80:83], v[24:27], v[216:219], v[80:83]\n"
    "ds_read_b64_tr_b16 v[20:21], v7 offset:32768\n"
    "v_mfma_f32_16x16x32_bf16 v[84:87], v[24:27], v[220:223], v[84:87]\n"
    "ds_read_b64_tr_b16 v[22:23], v7 offset:34816\n"
    "s_waitcnt lgkmcnt(4)\n"
    "v_mfma_f32_16x16x32_bf16 v[92:95], v[28:31], v[220:223], v[92:95]\n"
    "ds_read_b64_tr_b16 v[24:25], v8 offset:32768\n"
    "v_mfma_f32_16x16x32_bf16 v[88:91], v[28:31], v[216:219], v[88:91]\n"
    "ds_read_b64_tr_b16 v[26:27], v8 offset:34816\n"
    "s_cbranch_vccz .Lfz_RET_bY53\n"
    "s_barrier\n"
    ".Lfz_RET_bY53:\n"
    "v_mov_b32 v14, 0\n"
    "s_mov_b64 exec, 1\n"
    "buffer_atomic_or v14, off, s[44:47], s6 offset:16 sc0\n"
    "s_mov_b64 exec, -1\n"
    "s_add_u32 m0, s52, 0x18000\n"
    "s_lshl_b32 s87, s83, 16\n"
    "buffer_load_dwordx4 v9, s[12:15], s87 offen lds\n"
    "s_lshl_b32 s87, s48, 11\n"
    "v_lshl_add_u32 v1, v0, 4, s87\n"
    "v_and_b32 v32, 15, v0\n"
    "s_lshl_b32 s87, s84, 5\n"
    "v_add_u32 v32, s87, v32\n"
    "v_bfe_u32 v33, v0, 1, 2\n"
    "v_xor_b32 v33, s85, v33\n"
    "v_lshrrev_b32 v34, 4, v0\n"
    "v_lshlrev_b32 v34, 3, v34\n"
    "v_lshl_or_b32 v33, v33, 5, v34\n"
    "v_lshl_or_b32 v3, v32, 7, v33\n"
    "v_lshl_or_b32 v36, v32, 10, v34\n"
    "s_lshl_b32 s87, s83, 16\n"
    "s_lshl_b32 s94, s82, 7\n"
    "s_add_u32 s87, s87, s94\n"
    "s_lshl_b32 s94, s85, 5\n"
    "s_add_u32 s87, s87, s94\n"
    "v_add_u32 v36, s87, v36\n"
    "s_cbranch_vccnz .Lfz_RET_bY54\n"
    "s_barrier\n"
    ".Lfz_RET_bY54:\n"
    "s_waitcnt vmcnt(9) lgkmcnt(4)\n"
    "v_mfma_f32_16x16x32_bf16 v[96:99], v[16:19], v[224:227], v[96:99]\n"
    "ds_read_b64_tr_b16 v[28:29], v4 offset:32768\n"
    "s_waitcnt vmcnt(8)\n"
    "v_mfma_f32_16x16x32_bf16 v[100:103], v[16:19], v[228:231], v[100:103]\n"
    "ds_read_b64_tr_b16 v[30:31], v4 offset:34816\n"
    "s_waitcnt lgkmcnt(4)\n"
    "v_mfma_f32_16x16x32_bf16 v[108:111], v[20:23], v[228:231], v[108:111]\n"
    "ds_read_b64_tr_b16 v[16:17], v6 offset:49152\n"
    "v_mfma_f32_16x16x32_bf16 v[104:107], v[20:23], v[224:227], v[104:107]\n"
    "ds_read_b64_tr_b16 v[18:19], v6 offset:51200\n"
    "s_waitcnt lgkmcnt(4)\n"
    "v_mfma_f32_16x16x32_bf16 v[80:83], v[24:27], v[224:227], v[80:83]\n"
    "ds_read_b64_tr_b16 v[20:21], v7 offset:49152\n"
    "v_mfma_f32_16x16x32_bf16 v[84:87], v[24:27], v[228:231], v[84:87]\n"
    "ds_read_b64_tr_b16 v[22:23], v7 offset:51200\n"
    "s_waitcnt lgkmcnt(4)\n"
    "v_mfma_f32_16x16x32_bf16 v[92:95], v[28:31], v[228:231], v[92:95]\n"
    "ds_read_b64_tr_b16 v[24:25], v8 offset:49152\n"
    "v_mfma_f32_16x16x32_bf16 v[88:91], v[28:31], v[224:227], v[88:91]\n"
    "ds_read_b64_tr_b16 v[26:27], v8 offset:51200\n"
    "s_waitcnt vmcnt(7) lgkmcnt(4)\n"
    "v_mfma_f32_16x16x32_bf16 v[96:99], v[16:19], v[232:235], v[96:99]\n"
    "ds_read_b64_tr_b16 v[28:29], v4 offset:49152\n"
    "s_waitcnt vmcnt(6)\n"
    "v_mfma_f32_16x16x32_bf16 v[100:103], v[16:19], v[236:239], v[100:103]\n"
    "ds_read_b64_tr_b16 v[30:31], v4 offset:51200\n"
    "s_waitcnt lgkmcnt(4)\n"
    "v_mfma_f32_16x16x32_bf16 v[108:111], v[20:23], v[236:239], v[108:111]\n"
    "v_add_u32 v6, 0x1c000, v6\n"
    "ds_read_b64_tr_b16 v[16:17], v6 offset:0\n"
    "v_mfma_f32_16x16x32_bf16 v[104:107], v[20:23], v[232:235], v[104:107]\n"
    "ds_read_b64_tr_b16 v[18:19], v6 offset:2048\n"
    "s_waitcnt lgkmcnt(4)\n"
    "v_mfma_f32_16x16x32_bf16 v[80:83], v[24:27], v[232:235], v[80:83]\n"
    "v_add_u32 v7, 0x1c000, v7\n"
    "ds_read_b64_tr_b16 v[20:21], v7 offset:0\n"
    "v_mfma_f32_16x16x32_bf16 v[84:87], v[24:27], v[236:239], v[84:87]\n"
    "ds_read_b64_tr_b16 v[22:23], v7 offset:2048\n"
    "s_waitcnt lgkmcnt(4)\n"
    "v_mfma_f32_16x16x32_bf16 v[92:95], v[28:31], v[236:239], v[92:95]\n"
    "v_add_u32 v8, 0x1c000, v8\n"
    "ds_read_b64_tr_b16 v[24:25], v8 offset:0\n"
    "v_mfma_f32_16x16x32_bf16 v[88:91], v[28:31], v[232:235], v[88:91]\n"
    "ds_read_b64_tr_b16 v[26:27], v8 offset:2048\n"
    "v_xor_b32 v2, 0x800, v1\n"
    "v_add_u32 v2, 0x4000, v2\n"
    "ds_write_b128 v2, v[104:107] offset:0\n"
    "ds_write_b128 v2, v[108:111] offset:1024\n"
    "v_xor_b32 v2, 0x1000, v1\n"
    "v_add_u32 v2, 0x0, v2\n"
    "ds_write_b128 v2, v[80:83] offset:0\n"
    "ds_write_b128 v2, v[84:87] offset:1024\n"
    "v_xor_b32 v2, 0x1800, v1\n"
    "v_add_u32 v2, 0x14000, v2\n"
    "ds_write_b128 v2, v[88:91] offset:0\n"
    "ds_write_b128 v2, v[92:95] offset:1024\n"
    "s_waitcnt vmcnt(0) lgkmcnt(0)\n"
    "s_cbranch_vccz .Lfz_RET_bY55\n"
    "s_barrier\n"
    ".Lfz_RET_bY55:\n"
    "v_readfirstlane_b32 s90, v14\n"
    "s_sub_u32 s90, s90, s53\n"
    "s_cmp_eq_u32 s90, 4\n"
    "s_cbranch_scc0 .Lfz_SLOW_Y_5\n"
    ".Lfz_RET_Y_5:\n"
    "s_mov_b32 s88, 0x1c000\n"
    "buffer_load_dwordx4 v[240:243], v11, s[44:47], s88 offen offset:0 sc1\n"
    "s_mov_b32 s88, 0x1c000\n"
    "buffer_load_dwordx4 v[244:247], v11, s[44:47], s88 offen offset:1024 sc1\n"
    "s_mov_b32 s88, 0x1e000\n"
    "buffer_load_dwordx4 v[248:251], v11, s[44:47], s88 offen offset:0 sc1\n"
    "s_mov_b32 s88, 0x1e000\n"
    "buffer_load_dwordx4 v[252:255], v11, s[44:47], s88 offen offset:1024 sc1\n"
    "s_cbranch_vccnz .Lfz_RET_bY56\n"
    "s_barrier\n"
    ".Lfz_RET_bY56:\n"
    "s_waitcnt vmcnt(3)\n"
    "v_mfma_f32_16x16x32_bf16 v[112:115], v[16:19], v[240:243], 0\n"
    "v_add_u32 v4, 0x1c000, v4\n"
    "ds_read_b64_tr_b16 v[28:29], v4 offset:0\n"
    "s_waitcnt vmcnt(2)\n"
    "v_mfma_f32_16x16x32_bf16 v[116:119], v[16:19], v[244:247], 0\n"
    "ds_read_b64_tr_b16 v[30:31], v4 offset:2048\n"
    "ds_read_b128 v[104:107], v1 offset:16384\n"
    "v_mfma_f32_16x16x32_bf16 v[124:127], v[20:23], v[244:247], 0\n"
    "ds_read_b64_tr_b16 v[16:17], v6 offset:16384\n"
    "v_mfma_f32_16x16x32_bf16 v[120:123], v[20:23], v[240:243], 0\n"
    "ds_read_b64_tr_b16 v[18:19], v6 offset:18432\n"
    "ds_read_b128 v[108:111], v1 offset:17408\n"
    "v_mfma_f32_16x16x32_bf16 v[64:67], v[24:27], v[240:243], 0\n"
    "ds_read_b64_tr_b16 v[20:21], v7 offset:16384\n"
    "v_mfma_f32_16x16x32_bf16 v[68:71], v[24:27], v[244:247], 0\n"
    "ds_read_b64_tr_b16 v[22:23], v7 offset:18432\n"
    "ds_read_b128 v[80:83], v1 offset:0\n"
    "s_waitcnt lgkmcnt(7)\n"
    "v_mfma_f32_16x16x32_bf16 v[76:79], v[28:31], v[244:247], 0\n"
    "ds_read_b64_tr_b16 v[24:25], v8 offset:16384\n"
    "v_mfma_f32_16x16x32_bf16 v[72:75], v[28:31], v[240:243], 0\n"
    "ds_read_b64_tr_b16 v[26:27], v8 offset:18432\n"
    "ds_read_b128 v[84:87], v1 offset:1024\n"
    "s_waitcnt vmcnt(1) lgkmcnt(7)\n"
    "v_mfma_f32_16x16x32_bf16 v[112:115], v[16:19], v[248:251], v[112:115]\n"
    "ds_read_b64_tr_b16 v[28:29], v4 offset:16384\n"
    "s_waitcnt vmcnt(0)\n"
    "v_mfma_f32_16x16x32_bf16 v[116:119], v[16:19], v[252:255], v[116:119]\n"
    "ds_read_b64_tr_b16 v[30:31], v4 offset:18432\n"
    "v_add_u32 v1, 0x14000, v1\n"
    "ds_read_b128 v[88:91], v1 offset:0\n"
    "s_waitcnt lgkmcnt(7)\n"
    "v_mfma_f32_16x16x32_bf16 v[124:127], v[20:23], v[252:255], v[124:127]\n"
    "ds_read_b64_tr_b16 v[16:17], v6 offset:32768\n"
    "v_mfma_f32_16x16x32_bf16 v[120:123], v[20:23], v[248:251], v[120:123]\n"
    "ds_read_b64_tr_b16 v[18:19], v6 offset:34816\n"
    "ds_read_b128 v[92:95], v1 offset:1024\n"
    "s_waitcnt lgkmcnt(7)\n"
    "v_mfma_f32_16x16x32_bf16 v[64:67], v[24:27], v[248:251], v[64:67]\n"
    "ds_read_b64_tr_b16 v[20:21], v7 offset:32768\n"
    "v_mfma_f32_16x16x32_bf16 v[68:71], v[24:27], v[252:255], v[68:71]\n"
    "ds_read_b64_tr_b16 v[22:23], v7 offset:34816\n"
    "v_add_u32 v3, 0x18000, v3\n"
    "ds_read_b64 v[32:33], v3 offset:0\n"
    "s_waitcnt lgkmcnt(7)\n"
    "v_mfma_f32_16x16x32_bf16 v[76:79], v[28:31], v[252:255], v[76:79]\n"
    "ds_read_b64_tr_b16 v[24:25], v8 offset:32768\n"
    "v_mfma_f32_16x16x32_bf16 v[72:75], v[28:31], v[248:251], v[72:75]\n"
    "ds_read_b64_tr_b16 v[26:27], v8 offset:34816\n"
    "ds_read_b64 v[34:35], v3 offset:2048\n"
    "s_cbranch_vccz .Lfz_RET_bY57\n"
    "s_barrier\n"
    ".Lfz_RET_bY57:\n"
    "s_mov_b32 s88, 0x20000\n"
    "buffer_load_dwordx4 v[128:131], v11, s[44:47], s88 offen offset:0 sc1\n"
    "v_add_f32 v96, v96, v104\n"
    "v_add_f32 v97, v97, v105\n"
    "v_add_f32 v98, v98, v106\n"
    "v_add_f32 v99, v99, v107\n"
    "v_add_f32 v100, v100, v108\n"
    "v_add_f32 v101, v101, v109\n"
    "v_add_f32 v102, v102, v110\n"
    "v_add_f32 v103, v103, v111\n"
    "v_add_f32 v96, v96, v80\n"
    "v_add_f32 v97, v97, v81\n"
    "v_add_f32 v98, v98, v82\n"
    "v_add_f32 v99, v99, v83\n"
    "v_add_f32 v100, v100, v84\n"
    "v_add_f32 v101, v101, v85\n"
    "v_add_f32 v102, v102, v86\n"
    "v_add_f32 v103, v103, v87\n"
    "s_waitcnt lgkmcnt(9)\n"
    "v_add_f32 v96, v96, v88\n"
    "v_add_f32 v97, v97, v89\n"
    "v_add_f32 v98, v98, v90\n"
    "v_add_f32 v99, v99, v91\n"
    "s_waitcnt lgkmcnt(6)\n"
    "v_add_f32 v100, v100, v92\n"
    "v_add_f32 v101, v101, v93\n"
    "s_mov_b32 s88, 0x20000\n"
    "buffer_load_dwordx4 v[132:135], v11, s[44:47], s88 offen offset:1024 sc1\n"
    "v_add_f32 v102, v102, v94\n"
    "v_add_f32 v103, v103, v95\n"
    "v_mul_f32 v104, 0xbfb8aa3b, v96\n"
    "v_mul_f32 v105, 0xbfb8aa3b, v97\n"
    "v_mul_f32 v106, 0xbfb8aa3b, v98\n"
    "v_mul_f32 v107, 0xbfb8aa3b, v99\n"
    "v_mul_f32 v108, 0xbfb8aa3b, v100\n"
    "v_mul_f32 v109, 0xbfb8aa3b, v101\n"
    "v_mul_f32 v110, 0xbfb8aa3b, v102\n"
    "v_mul_f32 v111, 0xbfb8aa3b, v103\n"
    "v_exp_f32 v104, v104\n"
    "v_exp_f32 v105, v105\n"
    "v_exp_f32 v106, v106\n"
    "v_exp_f32 v107, v107\n"
    "v_exp_f32 v108, v108\n"
    "v_exp_f32 v109, v109\n"
    "s_mov_b32 s88, 0x22000\n"
    "buffer_load_dwordx4 v[136:139], v11, s[44:47], s88 offen offset:0 sc1\n"
    "v_exp_f32 v110, v110\n"
    "v_exp_f32 v111, v111\n"
    "v_add_f32 v104, 1.0, v104\n"
    "v_add_f32 v105, 1.0, v105\n"
    "v_add_f32 v106, 1.0, v106\n"
    "v_add_f32 v107, 1.0, v107\n"
    "v_add_f32 v108, 1.0, v108\n"
    "v_add_f32 v109, 1.0, v109\n"
    "v_add_f32 v110, 1.0, v110\n"
    "v_add_f32 v111, 1.0, v111\n"
    "v_rcp_f32 v104, v104\n"
    "v_rcp_f32 v105, v105\n"
    "v_rcp_f32 v106, v106\n"
    "v_rcp_f32 v107, v107\n"
    "v_rcp_f32 v108, v108\n"
    "s_mov_b32 s88, 0x22000\n"
    "buffer_load_dwordx4 v[140:143], v11, s[44:47], s88 offen offset:1024 sc1\n"
    "v_rcp_f32 v109, v109\n"
    "v_rcp_f32 v110, v110\n"
    "v_rcp_f32 v111, v111\n"
    "v_mul_f32 v96, v96, v104\n"
    "v_mul_f32 v97, v97, v105\n"
    "v_mul_f32 v98, v98, v106\n"
    "v_mul_f32 v99, v99, v107\n"
    "v_mul_f32 v100, v100, v108\n"
    "v_mul_f32 v101, v101, v109\n"
    "v_mul_f32 v102, v102, v110\n"
    "v_mul_f32 v103, v103, v111\n"
    "s_waitcnt lgkmcnt(3)\n"
    "v_lshlrev_b32 v80, 16, v32\n"
    "v_and_b32 v81, 0xffff0000, v32\n"
    "v_lshlrev_b32 v82, 16, v33\n"
    "v_and_b32 v83, 0xffff0000, v33\n"
    "s_waitcnt lgkmcnt(0)\n"
    "v_lshlrev_b32 v84, 16, v34\n"
    "v_and_b32 v85, 0xffff0000, v34\n"
    "v_lshlrev_b32 v86, 16, v35\n"
    "v_and_b32 v87, 0xffff0000, v35\n"
    "s_cbranch_vccnz .Lfz_RET_bY58\n"
    "s_barrier\n"
    ".Lfz_RET_bY58:\n"
    "s_waitcnt vmcnt(3)\n"
    "v_mfma_f32_16x16x32_bf16 v[112:115], v[16:19], v[128:131], v[112:115]\n"
    "ds_read_b64_tr_b16 v[28:29], v4 offset:32768\n"
    "s_waitcnt vmcnt(2)\n"
    "v_mfma_f32_16x16x32_bf16 v[116:119], v[16:19], v[132:135], v[116:119]\n"
    "ds_read_b64_tr_b16 v[30:31], v4 offset:34816\n"
    "v_mfma_f32_16x16x32_bf16 v[124:127], v[20:23], v[132:135], v[124:127]\n"
    "v_add_u32 v6, 0xffff4000, v6\n"
    "ds_read_b64_tr_b16 v[16:17], v6 offset:0\n"
    "v_mfma_f32_16x16x32_bf16 v[120:123], v[20:23], v[128:131], v[120:123]\n"
    "ds_read_b64_tr_b16 v[18:19], v6 offset:2048\n"
    "v_mfma_f32_16x16x32_bf16 v[64:67], v[24:27], v[128:131], v[64:67]\n"
    "v_add_u32 v7, 0xffff4000, v7\n"
    "ds_read_b64_tr_b16 v[20:21], v7 offset:0\n"
    "v_mfma_f32_16x16x32_bf16 v[68:71], v[24:27], v[132:135], v[68:71]\n"
    "ds_read_b64_tr_b16 v[22:23], v7 offset:2048\n"
    "s_waitcnt lgkmcnt(4)\n"
    "v_mfma_f32_16x16x32_bf16 v[76:79], v[28:31], v[132:135], v[76:79]\n"
    "v_add_u32 v8, 0xffff4000, v8\n"
    "ds_read_b64_tr_b16 v[24:25], v8 offset:0\n"
    "v_mfma_f32_16x16x32_bf16 v[72:75], v[28:31], v[128:131], v[72:75]\n"
    "ds_read_b64_tr_b16 v[26:27], v8 offset:2048\n"
    "s_waitcnt vmcnt(1) lgkmcnt(4)\n"
    "v_mfma_f32_16x16x32_bf16 v[112:115], v[16:19], v[136:139], v[112:115]\n"
    "v_add_u32 v4, 0xffff4000, v4\n"
    "ds_read_b64_tr_b16 v[28:29], v4 offset:0\n"
    "s_waitcnt vmcnt(0)\n"
    "v_mfma_f32_16x16x32_bf16 v[116:119], v[16:19], v[140:143], v[116:119]\n"
    "ds_read_b64_tr_b16 v[30:31], v4 offset:2048\n"
    "s_waitcnt lgkmcnt(4)\n"
    "v_mfma_f32_16x16x32_bf16 v[124:127], v[20:23], v[140:143], v[124:127]\n"
    "v_mfma_f32_16x16x32_bf16 v[120:123], v[20:23], v[136:139], v[120:123]\n"
    "s_waitcnt lgkmcnt(2)\n"
    "v_mfma_f32_16x16x32_bf16 v[64:67], v[24:27], v[136:139], v[64:67]\n"
    "v_mfma_f32_16x16x32_bf16 v[68:71], v[24:27], v[140:143], v[68:71]\n"
    "s_waitcnt lgkmcnt(0)\n"
    "v_mfma_f32_16x16x32_bf16 v[76:79], v[28:31], v[140:143], v[76:79]\n"
    "v_mfma_f32_16x16x32_bf16 v[72:75], v[28:31], v[136:139], v[72:75]\n"
    "s_cbranch_vccz .Lfz_RET_bY59\n"
    "s_barrier\n"
    ".Lfz_RET_bY59:\n"
    "v_xor_b32 v2, 0x800, v1\n"
    "v_add_u32 v2, 0xffff8000, v2\n"
    "ds_write_b128 v2, v[120:123] offset:0\n"
    "ds_write_b128 v2, v[124:127] offset:1024\n"
    "v_xor_b32 v2, 0x1000, v1\n"
    "v_add_u32 v2, 0xffff4000, v2\n"
    "ds_write_b128 v2, v[64:67] offset:0\n"
    "ds_write_b128 v2, v[68:71] offset:1024\n"
    "v_xor_b32 v2, 0x1800, v1\n"
    "v_add_u32 v2, 0xc000, v2\n"
    "ds_write_b128 v2, v[72:75] offset:0\n"
    "ds_write_b128 v2, v[76:79] offset:1024\n"
    "s_waitcnt lgkmcnt(0)\n"
    "s_barrier\n"
    "v_add_u32 v1, 0xffff8000, v1\n"
    "ds_read_b128 v[120:123], v1 offset:0\n"
    "ds_read_b128 v[124:127], v1 offset:1024\n"
    "v_add_u32 v1, 0xffffc000, v1\n"
    "ds_read_b128 v[64:67], v1 offset:0\n"
    "ds_read_b128 v[68:71], v1 offset:1024\n"
    "v_add_u32 v1, 0x18000, v1\n"
    "ds_read_b128 v[72:75], v1 offset:0\n"
    "ds_read_b128 v[76:79], v1 offset:1024\n"
    "s_waitcnt lgkmcnt(5)\n"
    "v_pk_add_f32 v[112:113], v[112:113], v[120:121]\n"
    "v_pk_add_f32 v[114:115], v[114:115], v[122:123]\n"
    "s_waitcnt lgkmcnt(4)\n"
    "v_pk_add_f32 v[116:117], v[116:117], v[124:125]\n"
    "v_pk_add_f32 v[118:119], v[118:119], v[126:127]\n"
    "s_waitcnt lgkmcnt(3)\n"
    "v_pk_add_f32 v[112:113], v[112:113], v[64:65]\n"
    "v_pk_add_f32 v[114:115], v[114:115], v[66:67]\n"
    "s_waitcnt lgkmcnt(2)\n"
    "v_pk_add_f32 v[116:117], v[116:117], v[68:69]\n"
    "v_pk_add_f32 v[118:119], v[118:119], v[70:71]\n"
    "s_waitcnt lgkmcnt(1)\n"
    "v_pk_add_f32 v[112:113], v[112:113], v[72:73]\n"
    "v_pk_add_f32 v[114:115], v[114:115], v[74:75]\n"
    "s_waitcnt lgkmcnt(0)\n"
    "v_pk_add_f32 v[116:117], v[116:117], v[76:77]\n"
    "v_pk_add_f32 v[118:119], v[118:119], v[78:79]\n"
    "v_pk_fma_f32 v[112:113], v[112:113], v[96:97], v[80:81]\n"
    "v_pk_fma_f32 v[114:115], v[114:115], v[98:99], v[82:83]\n"
    "v_pk_fma_f32 v[116:117], v[116:117], v[100:101], v[84:85]\n"
    "v_pk_fma_f32 v[118:119], v[118:119], v[102:103], v[86:87]\n"
    "v_cvt_pk_bf16_f32 v32, v112, v113\n"
    "v_cvt_pk_bf16_f32 v33, v114, v115\n"
    "v_cvt_pk_bf16_f32 v34, v116, v117\n"
    "v_cvt_pk_bf16_f32 v35, v118, v119\n"
    "s_movk_i32 s87, 0x4000\n"
    "buffer_store_dwordx2 v[32:33], v36, s[40:43], 0 offen\n"
    "buffer_store_dwordx2 v[34:35], v36, s[40:43], s87 offen\n"
    "s_cbranch_vccnz .Lfz_RET_bY60\n"
    "s_barrier\n"
    ".Lfz_RET_bY60:\n"
    "s_or_b32 s87, %2, s48\n"
    "s_cmp_eq_u32 s87, 0\n"
    "s_mov_b32 s94, 0xd68000\n"
    "s_cselect_b32 s87, s94, 0x40000000\n"
    "s_mov_b64 exec, 1\n"
    "buffer_store_dword v13, off, s[44:47], s87 sc1\n"
    "s_mov_b64 exec, -1\n"
    "s_endpgm\n"
    ".Lfz_SLOW_Y_0:\n"
    ".Lfz_SLOW_lY_0:\n"
    "s_cmp_eq_u32 s89, 0\n"
    "s_cbranch_scc1 .Lfz_RET_Y_0\n"
    "s_sub_u32 s89, s89, 1\n"
    "s_sleep 1\n"
    "v_mov_b32 v14, 0\n"
    "s_mov_b64 exec, 3\n"
    "buffer_atomic_or v14, v53, s[44:47], s11 offen sc0\n"
    "s_mov_b64 exec, -1\n"
    "s_waitcnt vmcnt(0)\n"
    "v_cmp_eq_u32 s[90:91], s53, v14\n"
    "s_nop 1\n"
    "s_and_b32 s90, s90, 3\n"
    "s_cmp_eq_u32 s90, 3\n"
    "s_cbranch_scc0 .Lfz_SLOW_lY_0\n"
    "s_branch .Lfz_RET_Y_0\n"
    ".Lfz_SLOW_Y_1:\n"
    ".Lfz_SLOW_lY_1:\n"
    "s_cmp_eq_u32 s89, 0\n"
    "s_cbranch_scc1 .Lfz_RET_Y_1\n"
    "s_sub_u32 s89, s89, 1\n"
    "s_sleep 1\n"
    "v_mov_b32 v14, 0\n"
    "s_mov_b64 exec, 1\n"
    "buffer_atomic_or v14, off, s[44:47], s6 offset:0 sc0\n"
    "s_mov_b64 exec, -1\n"
    "s_waitcnt vmcnt(0)\n"
    "v_readfirstlane_b32 s90, v14\n"
    "s_nop 1\n"
    "s_sub_u32 s90, s90, s53\n"
    "s_cmp_eq_u32 s90, 4\n"
    "s_cbranch_scc0 .Lfz_SLOW_lY_1\n"
    "s_branch .Lfz_RET_Y_1\n"
    ".Lfz_SLOW_Y_2:\n"
    ".Lfz_SLOW_lY_2:\n"
    "s_cmp_eq_u32 s89, 0\n"
    "s_cbranch_scc1 .Lfz_RET_Y_2\n"
    "s_sub_u32 s89, s89, 1\n"
    "s_sleep 1\n"
    "v_mov_b32 v14, 0\n"
    "s_mov_b64 exec, 1\n"
    "buffer_atomic_or v14, off, s[44:47], s6 offset:4 sc0\n"
    "s_mov_b64 exec, -1\n"
    "s_waitcnt vmcnt(0)\n"
    "v_readfirstlane_b32 s90, v14\n"
    "s_nop 1\n"
    "s_sub_u32 s90, s90, s53\n"
    "s_cmp_eq_u32 s90, 4\n"
    "s_cbranch_scc0 .Lfz_SLOW_lY_2\n"
    "s_branch .Lfz_RET_Y_2\n"
    ".Lfz_SLOW_Y_3:\n"
    ".Lfz_SLOW_lY_3:\n"
    "s_cmp_eq_u32 s89, 0\n"
    "s_cbranch_scc1 .Lfz_RET_Y_3\n"
    "s_sub_u32 s89, s89, 1\n"
    "s_sleep 1\n"
    "v_mov_b32 v14, 0\n"
    "s_mov_b64 exec, 1\n"
    "buffer_atomic_or v14, off, s[44:47], s6 offset:8 sc0\n"
    "s_mov_b64 exec, -1\n"
    "s_waitcnt vmcnt(0)\n"
    "v_readfirstlane_b32 s90, v14\n"
    "s_nop 1\n"
    "s_sub_u32 s90, s90, s53\n"
    "s_cmp_eq_u32 s90, 4\n"
    "s_cbranch_scc0 .Lfz_SLOW_lY_3\n"
    "s_branch .Lfz_RET_Y_3\n"
    ".Lfz_SLOW_Y_4:\n"
    ".Lfz_SLOW_lY_4:\n"
    "s_cmp_eq_u32 s89, 0\n"
    "s_cbranch_scc1 .Lfz_RET_Y_4\n"
    "s_sub_u32 s89, s89, 1\n"
    "s_sleep 1\n"
    "v_mov_b32 v14, 0\n"
    "s_mov_b64 exec, 1\n"
    "buffer_atomic_or v14, off, s[44:47], s6 offset:12 sc0\n"
    "s_mov_b64 exec, -1\n"
    "s_waitcnt vmcnt(0)\n"
    "v_readfirstlane_b32 s90, v14\n"
    "s_nop 1\n"
    "s_sub_u32 s90, s90, s53\n"
    "s_cmp_eq_u32 s90, 4\n"
    "s_cbranch_scc0 .Lfz_SLOW_lY_4\n"
    "s_branch .Lfz_RET_Y_4\n"
    ".Lfz_SLOW_Y_5:\n"
    ".Lfz_SLOW_lY_5:\n"
    "s_cmp_eq_u32 s89, 0\n"
    "s_cbranch_scc1 .Lfz_RET_Y_5\n"
    "s_sub_u32 s89, s89, 1\n"
    "s_sleep 1\n"
    "v_mov_b32 v14, 0\n"
    "s_mov_b64 exec, 1\n"
    "buffer_atomic_or v14, off, s[44:47], s6 offset:16 sc0\n"
    "s_mov_b64 exec, -1\n"
    "s_waitcnt vmcnt(0)\n"
    "v_readfirstlane_b32 s90, v14\n"
    "s_nop 1\n"
    "s_sub_u32 s90, s90, s53\n"
    "s_cmp_eq_u32 s90, 4\n"
    "s_cbranch_scc0 .Lfz_SLOW_lY_5\n"
    "s_branch .Lfz_RET_Y_5\n"
    ".Lfz_KEND:\n"
    : "+v"(tid)
    : "s"(__builtin_amdgcn_kernarg_segment_ptr()), "s"(blockIdx.x), "s"((unsigned)(size_t)lds)
    : "v1", "v2", "v3", "v4", "v5", "v6", "v7", "v8", "v9", "v10", "v11", "v12", "v13", "v14", "v15", "v16", "v17", "v18", "v19", "v20", "v21", "v22", "v23", "v24", "v25", "v26", "v27", "v28", "v29", "v30", "v31", "v32", "v33", "v34", "v35", "v36", "v37", "v38", "v39", "v40", "v41", "v42", "v43", "v44", "v45", "v46", "v47", "v48", "v49", "v50", "v51", "v52", "v53", "v54", "v55", "v56", "v57", "v58", "v59", "v60", "v61", "v62", "v63", "v64", "v65", "v66", "v67", "v68", "v69", "v70", "v71", "v72", "v73", "v74", "v75", "v76", "v77", "v78", "v79", "v80", "v81", "v82", "v83", "v84", "v85", "v86", "v87", "v88", "v89", "v90", "v91", "v92", "v93", "v94", "v95", "v96", "v97", "v98", "v99", "v100", "v101", "v102", "v103", "v104", "v105", "v106", "v107", "v108", "v109", "v110", "v111", "v112", "v113", "v114", "v115", "v116", "v117", "v118", "v119", "v120", "v121", "v122", "v123", "v124", "v125", "v126", "v127", "v128", "v129", "v130", "v131", "v132", "v133", "v134", "v135", "v136", "v137", "v138", "v139", "v140", "v141", "v142", "v143", "v144", "v145", "v146", "v147", "v148", "v149", "v150", "v151", "v152", "v153", "v154", "v155", "v156", "v157", "v158", "v159", "v160", "v161", "v162", "v163", "v164", "v165", "v166", "v167", "v168", "v169", "v170", "v171", "v172", "v173", "v174", "v175", "v176", "v177", "v178", "v179", "v180", "v181", "v182", "v183", "v184", "v185", "v186", "v187", "v188", "v189", "v190", "v191", "v192", "v193", "v194", "v195", "v196", "v197", "v198", "v199", "v200", "v201", "v202", "v203", "v204", "v205", "v206", "v207", "v208", "v209", "v210", "v211", "v212", "v213", "v214", "v215", "v216", "v217", "v218", "v219", "v220", "v221", "v222", "v223", "v224", "v225", "v226", "v227", "v228", "v229", "v230", "v231", "v232", "v233", "v234", "v235", "v236", "v237", "v238", "v239", "v240", "v241", "v242", "v243", "v244", "v245", "v246", "v247", "v248", "v249", "v250", "v251", "v252", "v253", "v254", "v255", "s4", "s5", "s6", "s7", "s8", "s9", "s10", "s11", "s12", "s13", "s14", "s15", "s16", "s17", "s18", "s19", "s20", "s21", "s22", "s23", "s24", "s25", "s26", "s27", "s28", "s29", "s30", "s31", "s32", "s33", "s34", "s35", "s36", "s37", "s38", "s39", "s40", "s41", "s42", "s43", "s44", "s45", "s46", "s47", "s48", "s49", "s50", "s51", "s52", "s53", "s54", "s55", "s56", "s57", "s58", "s59", "s60", "s61", "s62", "s63", "s64", "s65", "s66", "s67", "s68", "s69", "s70", "s71", "s72", "s73", "s74", "s75", "s76", "s77", "s78", "s79", "s80", "s81", "s82", "s83", "s84", "s85", "s86", "s87", "s88", "s89", "s90", "s91", "s92", "s93", "s94", "s95", "s96", "s97", "s98", "s99", "vcc", "scc", "memory");
}

extern "C" void kernel_launch(void* const* d_in, const int* in_sizes, int n_in,
                              void* d_out, int out_size, void* d_ws, size_t ws_size,
                              hipStream_t stream) {
  const unsigned F = 0x00020000u;
  Args a;
  a.x    = Buf{d_in[0], 2048u * 512u * 2u, F};
  a.wqkv = Buf{d_in[1], 512u * 1536u * 2u, F};
  a.wphi = Buf{d_in[2], 32u * 256u * 2u, F};
  a.wo   = Buf{d_in[3], 512u * 512u * 2u, F};
  a.wg   = Buf{d_in[5], 512u * 1792u * 2u, F};
  a.wu   = Buf{d_in[6], 512u * 1792u * 2u, F};
  a.wd   = Buf{d_in[7], 1792u * 512u * 2u, F};
  a.out  = Buf{d_out, 2048u * 512u * 2u, F};
  a.ws   = Buf{d_ws, 14061568u, F};
  fused_block<<<256, 512, 0, stream>>>(a);
}
```
